```python
import torch

def _softmax(x, axis=-1):
    return torch.softmax(x, dim=axis)
def _gelu(x):
    return torch.nn.functional.gelu(x, approximate="tanh")
def _silu(x):
    return torch.nn.functional.silu(x)
def _sigmoid(x):
    return torch.sigmoid(x)
def _layernorm(x, axis=-1, eps=1e-5):
    m = x.mean(dim=axis, keepdim=True)
    v = x.var(dim=axis, keepdim=True, unbiased=False)
    return (x - m) / torch.sqrt(v + eps)
def _scatter_add(tgt, idx, src, axis=0):
    return tgt.clone().index_add_(axis, idx, src)
def _segment_sum(src, seg, n):
    out = torch.zeros((n,) + src.shape[1:], dtype=src.dtype)
    return out.index_add_(0, seg, src)
def _pad(x, pw):
    flat = tuple(v for pair in reversed(pw) for v in pair)
    return torch.nn.functional.pad(x, flat)

kernel_name = 'sequential_preLN__ssm_conv_h3__moe_arch_mod__prompt'

def setup_inputs():

    _w = (lambda a: (lambda t: t.to(torch.bfloat16) if t.is_floating_point() else t)(torch.as_tensor(a)))
    def _A_setup():
        B, S, D = 1, 2048, 640
        ks = 4
        return {
            "q": _w((lambda *s: torch.randn(*s, dtype=torch.bfloat16) * 0.05)(B, S, D)),
            "k": _w((lambda *s: torch.randn(*s, dtype=torch.bfloat16) * 0.2)(B, S, D)),
            "v": _w((lambda *s: torch.randn(*s, dtype=torch.bfloat16) * 0.2)(B, S, D)),
            "shift_k": _w((lambda *s: torch.randn(*s, dtype=torch.bfloat16) * 0.2)(D, ks) * 0.5),
            "ssm_k": _w((lambda *s: torch.randn(*s, dtype=torch.bfloat16) * 0.8)(D, S) * (1.0 / S ** 0.5)),
            "ssm_D": _w((lambda *s: torch.randn(*s, dtype=torch.bfloat16) * 0.2)(D)),
        }
    _A_in = _A_setup()

    def _B_setup():
        B, S, D, F, C = 1, 2048, 640, 1728, 8
        return {
            "x": _w((lambda *s: torch.randn(*s, dtype=torch.bfloat16) * 0.05)(B, S, D)),
            "w_r": _w((lambda *s: torch.randn(*s, dtype=torch.bfloat16) * 0.05)(D)),
            "w_gate": _w((lambda *s: torch.randn(*s, dtype=torch.bfloat16) * 0.05)(D, F)),
            "w_up": _w((lambda *s: torch.randn(*s, dtype=torch.bfloat16) * 0.05)(D, F)),
            "w_down": _w((lambda *s: torch.randn(*s, dtype=torch.bfloat16) * 0.05)(F, D)),
        }
    _B_in = _B_setup()

    return {
        "A_q": _A_in["q"],
        "A_k": _A_in["k"],
        "A_v": _A_in["v"],
        "A_shift_k": _A_in["shift_k"],
        "A_ssm_k": _A_in["ssm_k"],
        "A_ssm_D": _A_in["ssm_D"],
        "B_x": _B_in["x"],
        "B_w_r": _B_in["w_r"],
        "B_w_gate": _B_in["w_gate"],
        "B_w_up": _B_in["w_up"],
        "B_w_down": _B_in["w_down"],
    }

def reference(A_q, A_k, A_v, A_shift_k, A_ssm_k, A_ssm_D, B_x, B_w_r, B_w_gate, B_w_up, B_w_down):
    def _A_ref(q, k, v, shift_k, ssm_k, ssm_D):
        B, S, D = q.shape
        ks = shift_k.shape[-1]
        k_pad = _pad(k, ((0, 0), (ks - 1, 0), (0, 0)))
        k_shift = torch.zeros_like(k)
        for t in range(ks):
            k_shift = k_shift + k_pad[:, t:t + S, :] * shift_k[:, ks - 1 - t][None, None, :]
        kv = k_shift * v
        L = 2 * S
        kv_pad = _pad(kv, ((0, 0), (0, S), (0, 0)))
        kf_pad = _pad(ssm_k, ((0, 0), (0, S)))
        kv_f = (lambda x, axes=(-2,-1): torch.fft.rfft2(x.float(), dim=axes))(kv_pad[:, None, :, :], axes=(1, 2))
        kf_f = (lambda x, axes=(-2,-1): torch.fft.rfft2(x.float(), dim=axes))(kf_pad[:, None, :], axes=(1, 2))
        kf_f = (lambda x, *d: x.permute(*d))(kf_f, 1, 2, 0)
        y_f = kv_f * kf_f[None, :, :, :]
        y = (lambda x, s, axes=(-2,-1): torch.fft.irfft2(x, s=s, dim=axes).to(torch.bfloat16))(y_f, (1, L), axes=(1, 2))[:, 0, :S, :]
        y = y + kv * ssm_D[None, None, :]
        return q * y

    def _B_ref(x, w_r, w_gate, w_up, w_down):
        B, S, D = x.shape
        C = 8
        r = x @ w_r
        idx = torch.argsort(-r, axis=-1)[:, :C]
        idx_e = (lambda x, n, axis: torch.repeat_interleave(x, n, dim=axis))(idx.reshape(B, C, 1), D, axis=2)
        x_sel = (lambda x, i, axis: torch.take_along_dim(x, i, dim=axis))(x, idx_e, axis=1)
        g = _silu(x_sel @ w_gate)
        u = x_sel @ w_up
        y = (g * u) @ w_down
        r_sel = _sigmoid((lambda x, i, axis: torch.take_along_dim(x, i, dim=axis))(r, idx, axis=1))
        y = y * r_sel.reshape(B, C, 1)
        b_off = (torch.arange(B) * S).reshape(B, 1)
        flat_idx = (idx + b_off).reshape(B * C)
        zero = (x * 0.0).reshape(B * S, D)
        upd = _scatter_add(zero, flat_idx, y.reshape(B * C, D), axis=0)
        return x + upd.reshape(B, S, D)

    x = A_q
    h = x + _A_ref(_layernorm(x, axis=-1), A_k, A_v, A_shift_k, A_ssm_k, A_ssm_D)
    h2 = h + _B_ref(_layernorm(h, axis=-1), B_w_r, B_w_gate, B_w_up, B_w_down)
    return h2
```

```cpp
#include <hip/hip_runtime.h>
#include <hip/hip_bf16.h>

typedef unsigned short u16;
using bf16x8 = __attribute__((ext_vector_type(8))) short;
using s16x4 = __attribute__((ext_vector_type(4))) short;
using f32x16 = __attribute__((ext_vector_type(16))) float;
using f32x4 = __attribute__((ext_vector_type(4))) float;

#define DI __device__ __forceinline__

static constexpr int S_ = 2048;
static constexpr int D_ = 640;
static constexpr int F_ = 1728;

DI float bf2f(unsigned x) { return __uint_as_float(x << 16); }
DI float lo2f(unsigned x) { return __uint_as_float(x << 16); }
DI float hi2f(unsigned x) { return __uint_as_float(x & 0xffff0000u); }
DI unsigned f2bf(float x) {
  __bf16 b = (__bf16)x;
  return (unsigned)__builtin_bit_cast(unsigned short, b);
}
DI float rb(float x) { return (float)((__bf16)x); }
DI unsigned pack2(float lo, float hi) { return f2bf(lo) | (f2bf(hi) << 16); }

template <int CTRL>
DI int dpp_i(int v) { return __builtin_amdgcn_update_dpp(0, v, CTRL, 0xF, 0xF, true); }
DI float wave_sum(float v) {
  v += __int_as_float(dpp_i<0xB1>(__float_as_int(v)));
  v += __int_as_float(dpp_i<0x4E>(__float_as_int(v)));
  v += __int_as_float(dpp_i<0x141>(__float_as_int(v)));
  v += __int_as_float(dpp_i<0x140>(__float_as_int(v)));
  float a = __int_as_float(__builtin_amdgcn_readlane(__float_as_int(v), 0));
  float b = __int_as_float(__builtin_amdgcn_readlane(__float_as_int(v), 16));
  float c = __int_as_float(__builtin_amdgcn_readlane(__float_as_int(v), 32));
  float d = __int_as_float(__builtin_amdgcn_readlane(__float_as_int(v), 48));
  return (a + b) + (c + d);
}
DI unsigned umax(unsigned a, unsigned b) { return a > b ? a : b; }
DI unsigned wave_umax(unsigned v) {
  v = umax(v, (unsigned)dpp_i<0xB1>((int)v));
  v = umax(v, (unsigned)dpp_i<0x4E>((int)v));
  v = umax(v, (unsigned)dpp_i<0x141>((int)v));
  v = umax(v, (unsigned)dpp_i<0x140>((int)v));
  unsigned a = (unsigned)__builtin_amdgcn_readlane((int)v, 0);
  unsigned b = (unsigned)__builtin_amdgcn_readlane((int)v, 16);
  unsigned c = (unsigned)__builtin_amdgcn_readlane((int)v, 32);
  unsigned d = (unsigned)__builtin_amdgcn_readlane((int)v, 48);
  return umax(umax(a, b), umax(c, d));
}

__global__ __launch_bounds__(128) void k1_prep(const u16* __restrict__ k, const u16* __restrict__ v,
                                               const u16* __restrict__ shift_k, u16* __restrict__ kvT) {
  const int bx = blockIdx.x;
  const int tt = bx & 31, ct = bx >> 5;
  const int tid = threadIdx.x;
  const int chunk = tid & 7, tg = tid >> 3;
  const int t = 64 * tt + 4 * tg;
  const int c = 64 * ct + 8 * chunk;
  uint4 kr[7], vr[4], sk[4];
#pragma unroll
  for (int i = 0; i < 7; ++i) {
    const int row = t - 3 + i;
    if (row >= 0) kr[i] = *(const uint4*)(k + (size_t)row * D_ + c);
    else kr[i] = make_uint4(0u, 0u, 0u, 0u);
  }
#pragma unroll
  for (int i = 0; i < 4; ++i) vr[i] = *(const uint4*)(v + (size_t)(t + i) * D_ + c);
#pragma unroll
  for (int i = 0; i < 4; ++i) sk[i] = *(const uint4*)(shift_k + (size_t)c * 4 + 8 * i);

#pragma unroll
  for (int j = 0; j < 8; ++j) {
    const unsigned* skp = (const unsigned*)sk;
    const unsigned s01 = skp[2 * j], s23 = skp[2 * j + 1];
    const float w0 = lo2f(s01), w1 = hi2f(s01), w2 = lo2f(s23), w3 = hi2f(s23);
    float kk[7], vv[4];
#pragma unroll
    for (int i = 0; i < 7; ++i) {
      const unsigned d = ((const unsigned*)&kr[i])[j >> 1];
      kk[i] = (j & 1) ? hi2f(d) : lo2f(d);
    }
#pragma unroll
    for (int i = 0; i < 4; ++i) {
      const unsigned d = ((const unsigned*)&vr[i])[j >> 1];
      vv[i] = (j & 1) ? hi2f(d) : lo2f(d);
    }
    float o[4];
#pragma unroll
    for (int i = 0; i < 4; ++i) {
      float ks = rb(kk[i] * w3);
      ks = rb(ks + rb(kk[i + 1] * w2));
      ks = rb(ks + rb(kk[i + 2] * w1));
      ks = rb(ks + rb(kk[i + 3] * w0));
      o[i] = ks * vv[i];
    }
    uint2 st;
    st.x = pack2(o[0], o[1]);
    st.y = pack2(o[2], o[3]);
    *(uint2*)(kvT + (size_t)(c + j) * S_ + t) = st;
  }
}

static constexpr int KC_STRIDE = 4160;
static constexpr int KC_BYTES = 8 * KC_STRIDE;
static constexpr int U_ROW = 80;
__global__ __launch_bounds__(256) void k2_conv(const u16* __restrict__ kvT, const u16* __restrict__ ssm_k,
                                               const u16* __restrict__ ssm_D, u16* __restrict__ y2T) {
  __shared__ __attribute__((aligned(16))) char lds[40960];
  char* kc = lds;
  char* us = lds + KC_BYTES;
  const int c = blockIdx.x;
  const int tid = threadIdx.x;
  const int lane = tid & 63, wave = tid >> 6;

  const uint4* kp = (const uint4*)(ssm_k + (size_t)c * S_);
  const int c0 = 254 - tid, c1 = 255 - tid;
  uint4 lo = make_uint4(0u, 0u, 0u, 0u);
  if (c0 >= 0) lo = kp[c0];
  const uint4 hi = kp[c1];
  const uint4 uu = ((const uint4*)(kvT + (size_t)c * S_))[tid];
  const float Dc = bf2f(ssm_D[c]);

  const uint4 z4 = make_uint4(0u, 0u, 0u, 0u);
  if (tid < 160) *(uint4*)(us + 16 * tid) = z4;
  if (tid < 32) *(uint4*)(kc + (tid >> 2) * KC_STRIDE + 16 * (256 + (tid & 3))) = z4;

  {
    const unsigned W[8] = {lo.x, lo.y, lo.z, lo.w, hi.x, hi.y, hi.z, hi.w};
    unsigned E[7], O[7];
#pragma unroll
    for (int d = 0; d < 7; ++d) {
      E[d] = (W[7 - d] >> 16) | (W[7 - d] << 16);
      O[d] = (W[7 - d] & 0xffffu) | (W[6 - d] & 0xffff0000u);
    }
    char* dst = kc + 16 * tid;
#pragma unroll
    for (int q = 0; q < 4; ++q) {
      *(uint4*)(dst + (7 - 2 * q) * KC_STRIDE) = make_uint4(E[q], E[q + 1], E[q + 2], E[q + 3]);
      *(uint4*)(dst + (6 - 2 * q) * KC_STRIDE) = make_uint4(O[q], O[q + 1], O[q + 2], O[q + 3]);
    }
  }
  *(uint4*)(us + ((tid >> 2) + 32) * U_ROW + 16 * (tid & 3)) = uu;
  __syncthreads();

  const int r = lane & 31, h = lane >> 5;
  const int m0 = 8 * wave;
  const char* pa = kc + (r & 7) * KC_STRIDE + 2 * (2040 - 8 * (r >> 3) + 8 * h) - 64 * m0;
  const char* pb = us + (r + 32 - m0) * U_ROW + 16 * h;
  f32x16 y0, y1;
#pragma unroll
  for (int i = 0; i < 16; ++i) { y0[i] = 0.f; y1[i] = 0.f; }
#pragma unroll
  for (int mm = 0; mm < 8; ++mm) {
#pragma unroll
    for (int e = 0; e < 2; ++e) {
      const bf16x8 a0 = *(const bf16x8*)(pa - 64 * mm + 32 * e);
      const bf16x8 a1 = *(const bf16x8*)(pa - 64 * mm + 32 * e - 2048);
      const bf16x8 b0 = *(const bf16x8*)(pb - U_ROW * mm + 32 * e);
      const bf16x8 b1 = *(const bf16x8*)(pb - U_ROW * mm + 32 * e + 32 * U_ROW);
      y0 = __builtin_amdgcn_mfma_f32_32x32x16_bf16(a0, b0, y0, 0, 0, 0);
      y1 = __builtin_amdgcn_mfma_f32_32x32x16_bf16(a0, b1, y1, 0, 0, 0);
      y1 = __builtin_amdgcn_mfma_f32_32x32x16_bf16(a1, b0, y1, 0, 0, 0);
    }
  }
  __syncthreads();
  {
    char* red = lds + wave * 9216 + r * 144 + 16 * h;
#pragma unroll
    for (int q = 0; q < 4; ++q) {
      *(f32x4*)(red + 32 * q) = (f32x4){y0[4 * q], y0[4 * q + 1], y0[4 * q + 2], y0[4 * q + 3]};
      *(f32x4*)(red + 32 * 144 + 32 * q) = (f32x4){y1[4 * q], y1[4 * q + 1], y1[4 * q + 2], y1[4 * q + 3]};
    }
  }
  __syncthreads();
  {
    const char* red = lds + (tid >> 2) * 144 + (tid & 3) * 32;
    f32x4 s0 = *(const f32x4*)(red), s1 = *(const f32x4*)(red + 16);
#pragma unroll
    for (int w = 1; w < 4; ++w) {
      s0 += *(const f32x4*)(red + w * 9216);
      s1 += *(const f32x4*)(red + w * 9216 + 16);
    }
    const unsigned U[4] = {uu.x, uu.y, uu.z, uu.w};
    float o[8];
#pragma unroll
    for (int j = 0; j < 8; ++j) {
      const float acc = j < 4 ? s0[j] : s1[j - 4];
      const float kv = (j & 1) ? hi2f(U[j >> 1]) : lo2f(U[j >> 1]);
      o[j] = rb(acc) + rb(kv * Dc);
    }
    uint4 st;
    st.x = pack2(o[0], o[1]); st.y = pack2(o[2], o[3]); st.z = pack2(o[4], o[5]); st.w = pack2(o[6], o[7]);
    *(uint4*)(y2T + (size_t)c * S_ + 8 * tid) = st;
  }
}

__global__ __launch_bounds__(512) void k3_token(const u16* __restrict__ x, const u16* __restrict__ y2T,
                                                const u16* __restrict__ w_r, u16* __restrict__ out,
                                                u16* __restrict__ hbuf, float2* __restrict__ stats,
                                                unsigned* __restrict__ keys) {
  __shared__ __attribute__((aligned(16))) u16 ys[8][D_ + 8];
  const int b = blockIdx.x;
  const int tile = (b & 7) * 32 + (b >> 3);
  const int t0 = tile * 8;
  const int tid = threadIdx.x, lane = tid & 63, wave = tid >> 6;
  const int t = t0 + wave;

  const uint4 a0 = *(const uint4*)(y2T + (size_t)tid * S_ + t0);
  uint4 a1 = make_uint4(0u, 0u, 0u, 0u);
  if (tid < 128) a1 = *(const uint4*)(y2T + (size_t)(512 + tid) * S_ + t0);
  unsigned xr[5], wr[5];
#pragma unroll
  for (int i = 0; i < 5; ++i) xr[i] = *(const unsigned*)(x + (size_t)t * D_ + 2 * lane + 128 * i);
#pragma unroll
  for (int i = 0; i < 5; ++i) wr[i] = *(const unsigned*)(w_r + 2 * lane + 128 * i);

  {
    const unsigned A[4] = {a0.x, a0.y, a0.z, a0.w};
#pragma unroll
    for (int j = 0; j < 8; ++j) ys[j][tid] = (u16)((j & 1) ? (A[j >> 1] >> 16) : (A[j >> 1] & 0xffffu));
    if (tid < 128) {
      const unsigned B[4] = {a1.x, a1.y, a1.z, a1.w};
#pragma unroll
      for (int j = 0; j < 8; ++j) ys[j][512 + tid] = (u16)((j & 1) ? (B[j >> 1] >> 16) : (B[j >> 1] & 0xffffu));
    }
  }
  float xf[10];
#pragma unroll
  for (int i = 0; i < 5; ++i) { xf[2 * i] = lo2f(xr[i]); xf[2 * i + 1] = hi2f(xr[i]); }
  float s = 0.f;
#pragma unroll
  for (int i = 0; i < 10; ++i) s += xf[i];
  const float mean1 = wave_sum(s) * (1.0f / D_);
  float q = 0.f;
#pragma unroll
  for (int i = 0; i < 10; ++i) { const float d = xf[i] - mean1; q += d * d; }
  const float var1 = wave_sum(q) * (1.0f / D_);
  const float m1 = rb(mean1);
  const float sd1 = rb(sqrtf(rb(rb(var1) + 1e-5f)));
  __syncthreads();
  float hf[10];
#pragma unroll
  for (int i = 0; i < 5; ++i) {
    const unsigned yy = *(const unsigned*)&ys[wave][2 * lane + 128 * i];
    const float ql0 = rb(rb(xf[2 * i] - m1) / sd1);
    const float ql1 = rb(rb(xf[2 * i + 1] - m1) / sd1);
    hf[2 * i] = rb(xf[2 * i] + rb(ql0 * lo2f(yy)));
    hf[2 * i + 1] = rb(xf[2 * i + 1] + rb(ql1 * hi2f(yy)));
  }
  s = 0.f;
#pragma unroll
  for (int i = 0; i < 10; ++i) s += hf[i];
  const float mean2 = wave_sum(s) * (1.0f / D_);
  q = 0.f;
#pragma unroll
  for (int i = 0; i < 10; ++i) { const float d = hf[i] - mean2; q += d * d; }
  const float var2 = wave_sum(q) * (1.0f / D_);
  const float m2 = rb(mean2);
  const float sd2 = rb(sqrtf(rb(rb(var2) + 1e-5f)));
  float dot = 0.f;
#pragma unroll
  for (int i = 0; i < 5; ++i) {
    const float hn0 = rb(rb(hf[2 * i] - m2) / sd2);
    const float hn1 = rb(rb(hf[2 * i + 1] - m2) / sd2);
    dot += hn0 * lo2f(wr[i]) + hn1 * hi2f(wr[i]);
    *(unsigned*)(out + (size_t)t * D_ + 2 * lane + 128 * i) = pack2(hf[2 * i] + hn0, hf[2 * i + 1] + hn1);
    *(unsigned*)(hbuf + (size_t)t * D_ + 2 * lane + 128 * i) = pack2(hf[2 * i], hf[2 * i + 1]);
  }
  const float rr = wave_sum(dot);
  if (lane == 0) {
    const unsigned rbits = f2bf(rr);
    const unsigned ord = (rbits & 0x8000u) ? (~rbits & 0xffffu) : (rbits | 0x8000u);
    keys[t] = (ord << 16) | (unsigned)(0xffff - t);
    stats[t] = make_float2(m2, sd2);
  }
}

typedef __attribute__((ext_vector_type(4))) short v4i16_t;
DI s16x4 tr_read(const char* p) {
  return __builtin_bit_cast(s16x4, __builtin_amdgcn_ds_read_tr16_b64_v4i16(
      (__attribute__((address_space(3))) v4i16_t*)p));
}

__global__ __launch_bounds__(256) void k4_gateup(const u16* __restrict__ w_gate, const u16* __restrict__ w_up,
                                                 const u16* __restrict__ hbuf, const float2* __restrict__ stats,
                                                 const unsigned* __restrict__ keys, u16* __restrict__ gu,
                                                 u16* __restrict__ hsel, u16* __restrict__ hnsel,
                                                 unsigned* __restrict__ selkeys) {
  __shared__ __attribute__((aligned(16))) char wl[D_ * 32];
  __shared__ __attribute__((aligned(16))) float red[4][8][16];
  __shared__ unsigned cand[32];
  const int b = blockIdx.x;
  const int slab = (b & 7) * 27 + (b >> 3);
  const int f0 = slab * 8;
  const int tid = threadIdx.x, lane = tid & 63, wave = tid >> 6;

  const int kk2 = min(tid + 512, D_ - 1);
  const uint4 wg0 = *(const uint4*)(w_gate + (size_t)tid * F_ + f0);
  const uint4 wu0 = *(const uint4*)(w_up + (size_t)tid * F_ + f0);
  const uint4 wg1 = *(const uint4*)(w_gate + (size_t)(tid + 256) * F_ + f0);
  const uint4 wu1 = *(const uint4*)(w_up + (size_t)(tid + 256) * F_ + f0);
  const uint4 wg2 = *(const uint4*)(w_gate + (size_t)kk2 * F_ + f0);
  const uint4 wu2 = *(const uint4*)(w_up + (size_t)kk2 * F_ + f0);
  {
    const uint4 ka = *(const uint4*)(keys + 8 * tid);
    const uint4 kb = *(const uint4*)(keys + 8 * tid + 4);
    unsigned kk[8] = {ka.x, ka.y, ka.z, ka.w, kb.x, kb.y, kb.z, kb.w};
#pragma unroll
    for (int rnd = 0; rnd < 8; ++rnd) {
      unsigned m = kk[0];
#pragma unroll
      for (int j = 1; j < 8; ++j) m = umax(m, kk[j]);
      const unsigned M = wave_umax(m);
#pragma unroll
      for (int j = 0; j < 8; ++j) kk[j] = (kk[j] == M) ? 0u : kk[j];
      if (lane == 0) cand[wave * 8 + rnd] = M;
    }
  }
  __syncthreads();
  unsigned sel[8];
  {
    unsigned kv = (lane < 32) ? cand[lane] : 0u;
#pragma unroll
    for (int rnd = 0; rnd < 8; ++rnd) {
      const unsigned M = wave_umax(kv);
      kv = (kv == M) ? 0u : kv;
      sel[rnd] = M;
    }
  }
  const int tok = lane & 15, g = lane >> 4;
  unsigned mykey = sel[0];
#pragma unroll
  for (int j = 1; j < 8; ++j) mykey = (tok == j) ? sel[j] : mykey;
  const int myidx = 0xffff - (int)(mykey & 0xffffu);
  uint4 hraw[5];
  const float2 st = stats[myidx];
#pragma unroll
  for (int i = 0; i < 5; ++i) hraw[i] = *(const uint4*)(hbuf + (size_t)myidx * D_ + 32 * (5 * wave + i) + 8 * g);
  *(uint4*)(wl + tid * 32) = wg0;
  *(uint4*)(wl + tid * 32 + 16) = wu0;
  *(uint4*)(wl + (tid + 256) * 32) = wg1;
  *(uint4*)(wl + (tid + 256) * 32 + 16) = wu1;
  if (tid < 128) {
    *(uint4*)(wl + (tid + 512) * 32) = wg2;
    *(uint4*)(wl + (tid + 512) * 32 + 16) = wu2;
  }
  __syncthreads();
  f32x4 acc = {0.f, 0.f, 0.f, 0.f};
  const int i16 = lane & 15;
#pragma unroll
  for (int i = 0; i < 5; ++i) {
    const int k0 = 32 * (5 * wave + i);
    const unsigned H[4] = {hraw[i].x, hraw[i].y, hraw[i].z, hraw[i].w};
    unsigned HN[4];
#pragma unroll
    for (int d = 0; d < 4; ++d) {
      const float a = rb(rb(lo2f(H[d]) - st.x) / st.y);
      const float bb = rb(rb(hi2f(H[d]) - st.x) / st.y);
      HN[d] = (tok < 8) ? pack2(a, bb) : 0u;
    }
    const uint4 hn4 = make_uint4(HN[0], HN[1], HN[2], HN[3]);
    if (b == 0 && tok < 8) {
      *(uint4*)(hsel + tok * D_ + k0 + 8 * g) = hraw[i];
      *(uint4*)(hnsel + tok * D_ + k0 + 8 * g) = hn4;
    }
    const bf16x8 afrag = __builtin_bit_cast(bf16x8, hn4);
    const char* p = wl + (k0 + 8 * g + (i16 >> 2)) * 32 + 8 * (i16 & 3);
    const s16x4 blo = tr_read(p);
    const s16x4 bhi = tr_read(p + 4 * 32);
    const bf16x8 bfrag = __builtin_shufflevector(blo, bhi, 0, 1, 2, 3, 4, 5, 6, 7);
    acc = __builtin_amdgcn_mfma_f32_16x16x32_bf16(afrag, bfrag, acc, 0, 0, 0);
  }
  if (b == 0 && tid < 8) selkeys[tid] = mykey;
  if (g < 2) {
#pragma unroll
    for (int j = 0; j < 4; ++j) red[wave][4 * g + j][i16] = acc[j];
  }
  __syncthreads();
  if (tid < 64) {
    const int tk = tid >> 3, col = tid & 7;
    const float G = rb((red[0][tk][col] + red[1][tk][col]) + (red[2][tk][col] + red[3][tk][col]));
    const float U = rb((red[0][tk][col + 8] + red[1][tk][col + 8]) + (red[2][tk][col + 8] + red[3][tk][col + 8]));
    const float gg = rb(G / (1.0f + __expf(-G)));
    gu[tk * F_ + f0 + col] = (u16)f2bf(gg * U);
  }
}

__global__ __launch_bounds__(256) void k5_down(const u16* __restrict__ w_down, const u16* __restrict__ gu,
                                               const u16* __restrict__ hsel, const u16* __restrict__ hnsel,
                                               const unsigned* __restrict__ selkeys, u16* __restrict__ out) {
  __shared__ __attribute__((aligned(16))) char wl[F_ * 16];
  __shared__ __attribute__((aligned(16))) float red[4][8][8];
  const int b = blockIdx.x;
  const int slab = (b & 7) * 10 + (b >> 3);
  const int c0 = slab * 8;
  const int tid = threadIdx.x, lane = tid & 63, wave = tid >> 6;
  uint4 wd[7];
#pragma unroll
  for (int i = 0; i < 7; ++i) {
    const int kk = min(tid + 256 * i, F_ - 1);
    wd[i] = *(const uint4*)(w_down + (size_t)kk * D_ + c0);
  }
  const int tok = lane & 15, g = lane >> 4;
  uint4 af[14];
#pragma unroll
  for (int i = 0; i < 14; ++i) {
    const int ks = wave + 4 * i;
    if (tok < 8 && ks < 54) af[i] = *(const uint4*)(gu + tok * F_ + 32 * ks + 8 * g);
    else af[i] = make_uint4(0u, 0u, 0u, 0u);
  }
  unsigned key = 0, hv = 0, hnv = 0;
  if (tid < 64) {
    key = selkeys[tid >> 3];
    hv = hsel[(tid >> 3) * D_ + c0 + (tid & 7)];
    hnv = hnsel[(tid >> 3) * D_ + c0 + (tid & 7)];
  }
#pragma unroll
  for (int i = 0; i < 7; ++i) {
    const int kk = tid + 256 * i;
    if (kk < F_) *(uint4*)(wl + kk * 16) = wd[i];
  }
  __syncthreads();
  f32x4 acc = {0.f, 0.f, 0.f, 0.f};
  const int i16 = lane & 15;
#pragma unroll
  for (int i = 0; i < 14; ++i) {
    const int ks = wave + 4 * i;
    if (ks < 54) {
      const int k0 = 32 * ks;
      const char* p = wl + (k0 + 8 * g + (i16 >> 2)) * 16 + 8 * (i16 & 1);
      const s16x4 blo = tr_read(p);
      const s16x4 bhi = tr_read(p + 4 * 16);
      const bf16x8 bfrag = __builtin_shufflevector(blo, bhi, 0, 1, 2, 3, 4, 5, 6, 7);
      acc = __builtin_amdgcn_mfma_f32_16x16x32_bf16(__builtin_bit_cast(bf16x8, af[i]), bfrag, acc, 0, 0, 0);
    }
  }
  if (g < 2 && i16 < 8) {
#pragma unroll
    for (int j = 0; j < 4; ++j) red[wave][4 * g + j][i16] = acc[j];
  }
  __syncthreads();
  if (tid < 64) {
    const int tk = tid >> 3, col = tid & 7;
    const float Y = rb((red[0][tk][col] + red[1][tk][col]) + (red[2][tk][col] + red[3][tk][col]));
    const unsigned ord = key >> 16;
    const unsigned rbits = (ord & 0x8000u) ? (ord & 0x7fffu) : (~ord & 0xffffu);
    const float rv = bf2f(rbits);
    const float rs = rb(1.0f / (1.0f + __expf(-rv)));
    const float y = rb(Y * rs);
    const float bo = rb(bf2f(hnv) + y);
    const int idx = 0xffff - (int)(key & 0xffffu);
    out[(size_t)idx * D_ + c0 + col] = (u16)f2bf(bf2f(hv) + bo);
  }
}

extern "C" void kernel_launch(void* const* d_in, const int* in_sizes, int n_in, void* d_out, int out_size,
                              void* d_ws, size_t ws_size, hipStream_t stream) {
  const u16* q = (const u16*)d_in[0];
  const u16* k = (const u16*)d_in[1];
  const u16* v = (const u16*)d_in[2];
  const u16* shift_k = (const u16*)d_in[3];
  const u16* ssm_k = (const u16*)d_in[4];
  const u16* ssm_D = (const u16*)d_in[5];
  const u16* w_r = (const u16*)d_in[7];
  const u16* w_gate = (const u16*)d_in[8];
  const u16* w_up = (const u16*)d_in[9];
  const u16* w_down = (const u16*)d_in[10];
  u16* out = (u16*)d_out;

  char* ws = (char*)d_ws;
  const size_t MAT = (size_t)S_ * D_ * 2;
  u16* kvT = (u16*)(ws);
  u16* y2T = (u16*)(ws + MAT);
  u16* hbuf = (u16*)(ws + 2 * MAT);
  char* p = ws + 3 * MAT;
  float2* stats = (float2*)p; p += S_ * 8;
  unsigned* keys = (unsigned*)p; p += S_ * 4;
  u16* gu = (u16*)p; p += 8 * F_ * 2;
  u16* hsel = (u16*)p; p += 8 * D_ * 2;
  u16* hnsel = (u16*)p; p += 8 * D_ * 2;
  unsigned* selkeys = (unsigned*)p; p += 64;

  k1_prep<<<320, 128, 0, stream>>>(k, v, shift_k, kvT);
  k2_conv<<<640, 256, 0, stream>>>(kvT, ssm_k, ssm_D, y2T);
  k3_token<<<256, 512, 0, stream>>>(q, y2T, w_r, out, hbuf, stats, keys);
  k4_gateup<<<216, 256, 0, stream>>>(w_gate, w_up, hbuf, stats, keys, gu, hsel, hnsel, selkeys);
  k5_down<<<80, 256, 0, stream>>>(w_down, gu, hsel, hnsel, selkeys, out);
}
```

```cpp
#include <hip/hip_runtime.h>
#include <hip/hip_bf16.h>

typedef unsigned short u16;
typedef unsigned long long u64;
using bf16x8 = __attribute__((ext_vector_type(8))) short;
using s16x4 = __attribute__((ext_vector_type(4))) short;
using f32x16 = __attribute__((ext_vector_type(16))) float;
using f32x4 = __attribute__((ext_vector_type(4))) float;
using u32x4 = __attribute__((ext_vector_type(4))) unsigned;
using u32x2 = __attribute__((ext_vector_type(2))) unsigned;
typedef __attribute__((address_space(1))) u64 gu64;

#define DI __device__ __forceinline__
#define RLX_AGENT __ATOMIC_RELAXED, __HIP_MEMORY_SCOPE_AGENT
#define AUX_SC1 16

static constexpr int S_ = 2048;
static constexpr int D_ = 640;
static constexpr int F_ = 1728;

static constexpr unsigned MAT = S_ * D_ * 2;
static constexpr unsigned WS_KVT = 0;
static constexpr unsigned WS_Y2T = MAT;
static constexpr unsigned WS_H = 2 * MAT;
static constexpr unsigned WS_CTL = 3 * MAT;
static constexpr unsigned WS_E = WS_CTL;
static constexpr unsigned WS_F1 = WS_CTL + 2048;
static constexpr unsigned WS_F2 = WS_CTL + 4096;
static constexpr unsigned WS_KEYS = WS_CTL + 8192;
static constexpr unsigned WS_F4 = WS_CTL + 24576;
static constexpr unsigned WS_STATS = WS_CTL + 32768;
static constexpr unsigned WS_GU = WS_CTL + 49152;
static constexpr unsigned WS_BYTES = WS_CTL + 49152 + 8 * F_ * 2;

static constexpr int KC_STRIDE = 4160;
static constexpr int KC_BYTES = 8 * KC_STRIDE;
static constexpr int U_ROW = 80;
static constexpr int TEAM = 40960;
static constexpr int L_W4 = 122880;
static constexpr int L_W5 = 81920;
static constexpr int L_TB = 143360;
static constexpr int L_CAND = 154880;
static constexpr int L_RED = 155392;
static constexpr int LDS_BYTES = 163840;

DI float bf2f(unsigned x) { return __uint_as_float(x << 16); }
DI float lo2f(unsigned x) { return __uint_as_float(x << 16); }
DI float hi2f(unsigned x) { return __uint_as_float(x & 0xffff0000u); }
DI unsigned f2bf(float x) {
  __bf16 b = (__bf16)x;
  return (unsigned)__builtin_bit_cast(unsigned short, b);
}
DI float rb(float x) { return (float)((__bf16)x); }
DI unsigned pack2(float lo, float hi) { return f2bf(lo) | (f2bf(hi) << 16); }

template <int CTRL>
DI int dpp_i(int v) { return __builtin_amdgcn_update_dpp(0, v, CTRL, 0xF, 0xF, true); }
DI float wave_sum(float v) {
  v += __int_as_float(dpp_i<0xB1>(__float_as_int(v)));
  v += __int_as_float(dpp_i<0x4E>(__float_as_int(v)));
  v += __int_as_float(dpp_i<0x141>(__float_as_int(v)));
  v += __int_as_float(dpp_i<0x140>(__float_as_int(v)));
  float a = __int_as_float(__builtin_amdgcn_readlane(__float_as_int(v), 0));
  float b = __int_as_float(__builtin_amdgcn_readlane(__float_as_int(v), 16));
  float c = __int_as_float(__builtin_amdgcn_readlane(__float_as_int(v), 32));
  float d = __int_as_float(__builtin_amdgcn_readlane(__float_as_int(v), 48));
  return (a + b) + (c + d);
}
DI unsigned umax(unsigned a, unsigned b) { return a > b ? a : b; }
DI unsigned wave_umax(unsigned v) {
  v = umax(v, (unsigned)dpp_i<0xB1>((int)v));
  v = umax(v, (unsigned)dpp_i<0x4E>((int)v));
  v = umax(v, (unsigned)dpp_i<0x141>((int)v));
  v = umax(v, (unsigned)dpp_i<0x140>((int)v));
  unsigned a = (unsigned)__builtin_amdgcn_readlane((int)v, 0);
  unsigned b = (unsigned)__builtin_amdgcn_readlane((int)v, 16);
  unsigned c = (unsigned)__builtin_amdgcn_readlane((int)v, 32);
  unsigned d = (unsigned)__builtin_amdgcn_readlane((int)v, 48);
  return umax(umax(a, b), umax(c, d));
}
typedef __attribute__((ext_vector_type(4))) short v4i16_t;
DI s16x4 tr_read(const char* p) {
  return __builtin_bit_cast(s16x4, __builtin_amdgcn_ds_read_tr16_b64_v4i16(
      (__attribute__((address_space(3))) v4i16_t*)p));
}
DI void drain() { asm volatile("s_waitcnt vmcnt(0)" ::: "memory"); }
static constexpr int SPIN_CAP = 1 << 20;

__global__ __launch_bounds__(1024) void fused_kernel(
    const u16* __restrict__ q, const u16* __restrict__ k, const u16* __restrict__ v,
    const u16* __restrict__ shift_k, const u16* __restrict__ ssm_k, const u16* __restrict__ ssm_D,
    const u16* __restrict__ w_r, const u16* __restrict__ w_gate, const u16* __restrict__ w_up,
    const u16* __restrict__ w_down, u16* out, char* ws) {
  __shared__ __attribute__((aligned(16))) char lds[LDS_BYTES];
  const int b = blockIdx.x;
  const int grp = b & 7, ib = b >> 3;
  const int tid = threadIdx.x, lane = tid & 63;
  const int wave = __builtin_amdgcn_readfirstlane(tid >> 6);
  const int team = wave >> 2, tt = tid & 255;
  const __amdgpu_buffer_rsrc_t rs = __builtin_amdgcn_make_buffer_rsrc(ws, 0, WS_BYTES, 0x00020000);
  const __amdgpu_buffer_rsrc_t ro = __builtin_amdgcn_make_buffer_rsrc(out, 0, MAT, 0x00020000);

  const u64 tag = __hip_atomic_load((gu64*)(ws + WS_E), RLX_AGENT) + 1ull;

  const int nch = (ib < 16) ? 3 : 2;
  const int ch = 80 * grp + ((ib < 16) ? 3 * ib : 48 + 2 * (ib - 16)) + team;
  const bool conv_on = team < nch;
  u32x4 klo = {0u, 0u, 0u, 0u}, khi = {0u, 0u, 0u, 0u};
  float Dc = 0.f;
  if (conv_on) {
    const u32x4* kp = (const u32x4*)(ssm_k + (size_t)ch * S_);
    if (tt < 255) klo = kp[254 - tt];
    khi = kp[255 - tt];
    Dc = bf2f(ssm_D[ch]);
  }
  const int t0 = (grp * 32 + ib) * 8;
  unsigned xr[5], wr[5];
  {
    const int t = t0 + (wave & 7);
#pragma unroll
    for (int i = 0; i < 5; ++i) xr[i] = *(const unsigned*)(q + (size_t)t * D_ + 2 * lane + 128 * i);
#pragma unroll
    for (int i = 0; i < 5; ++i) wr[i] = *(const unsigned*)(w_r + 2 * lane + 128 * i);
  }

  if (tid < 640) {
    const int tok = tid / 10, chunk = tid - 10 * tok;
    const int t = 64 * ib + tok;
    const int c = 80 * grp + 8 * chunk;
    u32x4 kr[4], sk[4];
#pragma unroll
    for (int i = 0; i < 4; ++i) {
      const int row = t - 3 + i;
      kr[i] = *(const u32x4*)(k + (size_t)(row < 0 ? 0 : row) * D_ + c);
      if (row < 0) kr[i] = (u32x4){0u, 0u, 0u, 0u};
    }
    const u32x4 vr = *(const u32x4*)(v + (size_t)t * D_ + c);
#pragma unroll
    for (int i = 0; i < 4; ++i) sk[i] = *(const u32x4*)(shift_k + (size_t)c * 4 + 8 * i);
#pragma unroll
    for (int j = 0; j < 8; ++j) {
      const unsigned s01 = sk[j >> 1][2 * (j & 1)], s23 = sk[j >> 1][2 * (j & 1) + 1];
      const float w0 = lo2f(s01), w1 = hi2f(s01), w2 = lo2f(s23), w3 = hi2f(s23);
      float kk[4];
#pragma unroll
      for (int i = 0; i < 4; ++i) kk[i] = (j & 1) ? hi2f(kr[i][j >> 1]) : lo2f(kr[i][j >> 1]);
      const float vv = (j & 1) ? hi2f(vr[j >> 1]) : lo2f(vr[j >> 1]);
      float ks = rb(kk[0] * w3);
      ks = rb(ks + rb(kk[1] * w2));
      ks = rb(ks + rb(kk[2] * w1));
      ks = rb(ks + rb(kk[3] * w0));
      *(u16*)(lds + L_TB + (8 * chunk + j) * 144 + 2 * tok) = (u16)f2bf(ks * vv);
    }
  } else if (wave >= 12) {
    if (b < 216) {
      const int f0 = ((b & 7) * 27 + (b >> 3)) * 8;
      for (int p = tt; p < 1280; p += 256) {
        const int row = p >> 1;
        const u16* src = ((p & 1) ? w_up : w_gate) + (size_t)row * F_ + f0;
        *(u32x4*)(lds + L_W4 + 16 * p) = *(const u32x4*)src;
      }
    }
  }
  if (b >= 216 && wave >= 8) {
    const int j = b - 216;
    const int c0 = 16 * ((j & 7) * 5 + (j >> 3));
    for (int p = tid - 512; p < 3456; p += 512) {
      *(u32x4*)(lds + L_W5 + 16 * p) = *(const u32x4*)(w_down + (size_t)(p >> 1) * D_ + c0 + 8 * (p & 1));
    }
  }
  __syncthreads();
  if (tid < 640) {
    const int row = tid >> 3, piece = tid & 7;
    const u32x4 d = *(const u32x4*)(lds + L_TB + row * 144 + 16 * piece);
    __builtin_amdgcn_raw_buffer_store_b128(d, rs, WS_KVT + ((80 * grp + row) * S_ + 64 * ib + 8 * piece) * 2, 0, AUX_SC1);
  }
  char* kc = lds + TEAM * team;
  char* us = kc + KC_BYTES;
  if (conv_on) {
    const u32x4 z4 = {0u, 0u, 0u, 0u};
    if (tt < 160) *(u32x4*)(us + 16 * tt) = z4;
    if (tt < 32) *(u32x4*)(kc + (tt >> 2) * KC_STRIDE + 16 * (256 + (tt & 3))) = z4;
    const unsigned W[8] = {klo[0], klo[1], klo[2], klo[3], khi[0], khi[1], khi[2], khi[3]};
    unsigned E[7], O[7];
#pragma unroll
    for (int d = 0; d < 7; ++d) {
      E[d] = (W[7 - d] >> 16) | (W[7 - d] << 16);
      O[d] = (W[7 - d] & 0xffffu) | (W[6 - d] & 0xffff0000u);
    }
    char* dst = kc + 16 * tt;
#pragma unroll
    for (int qq = 0; qq < 4; ++qq) {
      *(u32x4*)(dst + (7 - 2 * qq) * KC_STRIDE) = (u32x4){E[qq], E[qq + 1], E[qq + 2], E[qq + 3]};
      *(u32x4*)(dst + (6 - 2 * qq) * KC_STRIDE) = (u32x4){O[qq], O[qq + 1], O[qq + 2], O[qq + 3]};
    }
  }
  drain();
  __syncthreads();
  if (tid == 0) __hip_atomic_store((gu64*)(ws + WS_F1) + (grp * 32 + ib), tag, RLX_AGENT);

  float xf[10];
#pragma unroll
  for (int i = 0; i < 5; ++i) { xf[2 * i] = lo2f(xr[i]); xf[2 * i + 1] = hi2f(xr[i]); }
  float m1, sd1;
  {
    float s = 0.f;
#pragma unroll
    for (int i = 0; i < 10; ++i) s += xf[i];
    const float mean1 = wave_sum(s) * (1.0f / D_);
    float qq = 0.f;
#pragma unroll
    for (int i = 0; i < 10; ++i) { const float d = xf[i] - mean1; qq += d * d; }
    const float var1 = wave_sum(qq) * (1.0f / D_);
    m1 = rb(mean1);
    sd1 = rb(sqrtf(rb(rb(var1) + 1e-5f)));
  }

  if (wave == 0) {
    const gu64* f = (const gu64*)(ws + WS_F1) + grp * 32 + (lane & 31);
    for (int it = 0; it < SPIN_CAP; ++it) {
      const bool ok = __hip_atomic_load(f, RLX_AGENT) == tag;
      if (__all(ok)) break;
      __builtin_amdgcn_s_sleep(1);
    }
  }
  __syncthreads();
  u32x4 uu = {0u, 0u, 0u, 0u};
  if (conv_on) {
    uu = __builtin_amdgcn_raw_buffer_load_b128(rs, WS_KVT + (ch * S_ + 8 * tt) * 2, 0, AUX_SC1);
    *(u32x4*)(us + ((tt >> 2) + 32) * U_ROW + 16 * (tt & 3)) = uu;
  }
  __syncthreads();
  const int r = lane & 31, hh = lane >> 5;
  f32x16 y0, y1;
#pragma unroll
  for (int i = 0; i < 16; ++i) { y0[i] = 0.f; y1[i] = 0.f; }
  if (conv_on) {
    const int m0 = 8 * (wave & 3);
    const char* pa = kc + (r & 7) * KC_STRIDE + 2 * (2040 - 8 * (r >> 3) + 8 * hh) - 64 * m0;
    const char* pb = us + (r + 32 - m0) * U_ROW + 16 * hh;
#pragma unroll
    for (int mm = 0; mm < 8; ++mm) {
#pragma unroll
      for (int e = 0; e < 2; ++e) {
        const bf16x8 a0 = *(const bf16x8*)(pa - 64 * mm + 32 * e);
        const bf16x8 a1 = *(const bf16x8*)(pa - 64 * mm + 32 * e - 2048);
        const bf16x8 b0 = *(const bf16x8*)(pb - U_ROW * mm + 32 * e);
        const bf16x8 b1 = *(const bf16x8*)(pb - U_ROW * mm + 32 * e + 32 * U_ROW);
        y0 = __builtin_amdgcn_mfma_f32_32x32x16_bf16(a0, b0, y0, 0, 0, 0);
        y1 = __builtin_amdgcn_mfma_f32_32x32x16_bf16(a0, b1, y1, 0, 0, 0);
        y1 = __builtin_amdgcn_mfma_f32_32x32x16_bf16(a1, b0, y1, 0, 0, 0);
      }
    }
  }
  __syncthreads();
  if (conv_on) {
    char* red = kc + (wave & 3) * 9216 + r * 144 + 16 * hh;
#pragma unroll
    for (int qq = 0; qq < 4; ++qq) {
      *(f32x4*)(red + 32 * qq) = (f32x4){y0[4 * qq], y0[4 * qq + 1], y0[4 * qq + 2], y0[4 * qq + 3]};
      *(f32x4*)(red + 32 * 144 + 32 * qq) = (f32x4){y1[4 * qq], y1[4 * qq + 1], y1[4 * qq + 2], y1[4 * qq + 3]};
    }
  }
  __syncthreads();
  if (conv_on) {
    const char* red = kc + (tt >> 2) * 144 + (tt & 3) * 32;
    f32x4 s0 = *(const f32x4*)(red), s1 = *(const f32x4*)(red + 16);
#pragma unroll
    for (int w = 1; w < 4; ++w) {
      s0 += *(const f32x4*)(red + w * 9216);
      s1 += *(const f32x4*)(red + w * 9216 + 16);
    }
    float o[8];
#pragma unroll
    for (int j = 0; j < 8; ++j) {
      const float acc = j < 4 ? s0[j] : s1[j - 4];
      const float kv = (j & 1) ? hi2f(uu[j >> 1]) : lo2f(uu[j >> 1]);
      o[j] = rb(acc) + rb(kv * Dc);
    }
    const u32x4 st = {pack2(o[0], o[1]), pack2(o[2], o[3]), pack2(o[4], o[5]), pack2(o[6], o[7])};
    __builtin_amdgcn_raw_buffer_store_b128(st, rs, WS_Y2T + (ch * S_ + 8 * tt) * 2, 0, AUX_SC1);
  }
  drain();
  __syncthreads();
  if (tid == 0) __hip_atomic_store((gu64*)(ws + WS_F2) + b, tag, RLX_AGENT);

  if (wave == 0) {
    const gu64* f = (const gu64*)(ws + WS_F2) + lane;
    for (int it = 0; it < SPIN_CAP; ++it) {
      bool ok = true;
#pragma unroll
      for (int i = 0; i < 4; ++i) ok &= __hip_atomic_load(f + 64 * i, RLX_AGENT) == tag;
      if (__all(ok)) break;
      __builtin_amdgcn_s_sleep(1);
    }
  }
  __syncthreads();
  {
    u16* ys = (u16*)(lds + L_TB);
    if (tid < 640) {
      const u32x4 a = __builtin_amdgcn_raw_buffer_load_b128(rs, WS_Y2T + (tid * S_ + t0) * 2, 0, AUX_SC1);
#pragma unroll
      for (int j = 0; j < 8; ++j) ys[j * 648 + tid] = (u16)((j & 1) ? (a[j >> 1] >> 16) : (a[j >> 1] & 0xffffu));
    }
    __syncthreads();
    if (wave < 8) {
      const int t = t0 + wave;
      float hf[10];
#pragma unroll
      for (int i = 0; i < 5; ++i) {
        const unsigned yy = *(const unsigned*)&ys[wave * 648 + 2 * lane + 128 * i];
        const float ql0 = rb(rb(xf[2 * i] - m1) / sd1);
        const float ql1 = rb(rb(xf[2 * i + 1] - m1) / sd1);
        hf[2 * i] = rb(xf[2 * i] + rb(ql0 * lo2f(yy)));
        hf[2 * i + 1] = rb(xf[2 * i + 1] + rb(ql1 * hi2f(yy)));
      }
      float s = 0.f;
#pragma unroll
      for (int i = 0; i < 10; ++i) s += hf[i];
      const float mean2 = wave_sum(s) * (1.0f / D_);
      float qq = 0.f;
#pragma unroll
      for (int i = 0; i < 10; ++i) { const float d = hf[i] - mean2; qq += d * d; }
      const float var2 = wave_sum(qq) * (1.0f / D_);
      const float m2 = rb(mean2);
      const float sd2 = rb(sqrtf(rb(rb(var2) + 1e-5f)));
      float dot = 0.f;
      unsigned* stg = (unsigned*)(lds + wave * 2560);
#pragma unroll
      for (int i = 0; i < 5; ++i) {
        const float hn0 = rb(rb(hf[2 * i] - m2) / sd2);
        const float hn1 = rb(rb(hf[2 * i + 1] - m2) / sd2);
        dot += hn0 * lo2f(wr[i]) + hn1 * hi2f(wr[i]);
        stg[lane + 64 * i] = pack2(hf[2 * i] + hn0, hf[2 * i + 1] + hn1);
        stg[320 + lane + 64 * i] = pack2(hf[2 * i], hf[2 * i + 1]);
      }
      const float rr = wave_sum(dot);
      __builtin_amdgcn_fence(__ATOMIC_RELEASE, "wavefront");
      __builtin_amdgcn_wave_barrier();
      __builtin_amdgcn_fence(__ATOMIC_ACQUIRE, "wavefront");
      {
        const u32x4 o0 = *(const u32x4*)(stg + 4 * lane);
        const u32x4 h0 = *(const u32x4*)(stg + 320 + 4 * lane);
        __builtin_amdgcn_raw_buffer_store_b128(o0, ro, (t * D_ + 8 * lane) * 2, 0, AUX_SC1);
        __builtin_amdgcn_raw_buffer_store_b128(h0, rs, WS_H + (t * D_ + 8 * lane) * 2, 0, AUX_SC1);
        if (lane < 16) {
          const u32x4 o1 = *(const u32x4*)(stg + 256 + 4 * lane);
          const u32x4 h1 = *(const u32x4*)(stg + 320 + 256 + 4 * lane);
          __builtin_amdgcn_raw_buffer_store_b128(o1, ro, (t * D_ + 512 + 8 * lane) * 2, 0, AUX_SC1);
          __builtin_amdgcn_raw_buffer_store_b128(h1, rs, WS_H + (t * D_ + 512 + 8 * lane) * 2, 0, AUX_SC1);
        }
        if (lane == 0) {
          const u32x2 sv = {__float_as_uint(m2), __float_as_uint(sd2)};
          __builtin_amdgcn_raw_buffer_store_b64(sv, rs, WS_STATS + t * 8, 0, AUX_SC1);
        }
      }
      drain();
      if (lane == 0) __hip_atomic_store((gu64*)(ws + WS_KEYS) + t, (tag << 16) | (u64)f2bf(rr), RLX_AGENT);
    }
  }

  unsigned* cand = (unsigned*)(lds + L_CAND);
  {
    unsigned k0 = 0, k1 = 0;
    const unsigned tlo = (unsigned)(tag << 16), thi = (unsigned)(tag >> 16);
    for (int it = 0; it < SPIN_CAP; ++it) {
      const u32x4 gq = __builtin_amdgcn_raw_buffer_load_b128(rs, WS_KEYS + tid * 16, 0, AUX_SC1);
      const bool ok = ((gq[0] & 0xffff0000u) == tlo) && (gq[1] == thi) && ((gq[2] & 0xffff0000u) == tlo) && (gq[3] == thi);
      k0 = gq[0] & 0xffffu; k1 = gq[2] & 0xffffu;
      if (__all(ok)) break;
      __builtin_amdgcn_s_sleep(1);
    }
    k0 = (k0 & 0x8000u) ? (~k0 & 0xffffu) : (k0 | 0x8000u);
    k1 = (k1 & 0x8000u) ? (~k1 & 0xffffu) : (k1 | 0x8000u);
    k0 = (k0 << 16) | (unsigned)(0xffff - 2 * tid);
    k1 = (k1 << 16) | (unsigned)(0xffff - 2 * tid - 1);
#pragma unroll
    for (int rnd = 0; rnd < 8; ++rnd) {
      const unsigned M = wave_umax(umax(k0, k1));
      k0 = (k0 == M) ? 0u : k0;
      k1 = (k1 == M) ? 0u : k1;
      if (lane == 0) cand[wave * 8 + rnd] = M;
    }
  }
  __syncthreads();
  unsigned sel[8];
  {
    unsigned k0 = cand[lane], k1 = cand[lane + 64];
#pragma unroll
    for (int rnd = 0; rnd < 8; ++rnd) {
      const unsigned M = wave_umax(umax(k0, k1));
      k0 = (k0 == M) ? 0u : k0;
      k1 = (k1 == M) ? 0u : k1;
      sel[rnd] = M;
    }
  }
  const int tok = lane & 15, g = lane >> 4, i16 = lane & 15;
  float* red = (float*)(lds + L_RED);
  if (b < 216) {
    const int slab = (b & 7) * 27 + (b >> 3);
    const int f0 = slab * 8;
    if (wave < 10) {
      unsigned mykey = sel[0];
#pragma unroll
      for (int j = 1; j < 8; ++j) mykey = (tok == j) ? sel[j] : mykey;
      const int myidx = 0xffff - (int)(mykey & 0xffffu);
      const u32x2 stv = __builtin_amdgcn_raw_buffer_load_b64(rs, WS_STATS + myidx * 8, 0, AUX_SC1);
      u32x4 hraw[2];
#pragma unroll
      for (int i = 0; i < 2; ++i)
        hraw[i] = __builtin_amdgcn_raw_buffer_load_b128(rs, WS_H + (myidx * D_ + 32 * (2 * wave + i) + 8 * g) * 2, 0, AUX_SC1);
      const float sm = __uint_as_float(stv[0]), ss = __uint_as_float(stv[1]);
      f32x4 acc = {0.f, 0.f, 0.f, 0.f};
#pragma unroll
      for (int i = 0; i < 2; ++i) {
        const int k0 = 32 * (2 * wave + i);
        u32x4 HN;
#pragma unroll
        for (int d = 0; d < 4; ++d) {
          const float a = rb(rb(lo2f(hraw[i][d]) - sm) / ss);
          const float bb = rb(rb(hi2f(hraw[i][d]) - sm) / ss);
          HN[d] = (tok < 8) ? pack2(a, bb) : 0u;
        }
        const char* p = lds + L_W4 + (k0 + 8 * g + (i16 >> 2)) * 32 + 8 * (i16 & 3);
        const s16x4 blo = tr_read(p);
        const s16x4 bhi = tr_read(p + 4 * 32);
        const bf16x8 bfrag = __builtin_shufflevector(blo, bhi, 0, 1, 2, 3, 4, 5, 6, 7);
        acc = __builtin_amdgcn_mfma_f32_16x16x32_bf16(__builtin_bit_cast(bf16x8, HN), bfrag, acc, 0, 0, 0);
      }
      if (g < 2) {
#pragma unroll
        for (int j = 0; j < 4; ++j) red[(wave * 8 + 4 * g + j) * 16 + i16] = acc[j];
      }
    }
    __syncthreads();
    if (wave == 0) {
      const int tk = lane >> 3, col = lane & 7;
      float G = 0.f, U = 0.f;
#pragma unroll
      for (int w = 0; w < 10; ++w) {
        G += red[(w * 8 + tk) * 16 + col];
        U += red[(w * 8 + tk) * 16 + col + 8];
      }
      G = rb(G); U = rb(U);
      const float gg = rb(G / (1.0f + __expf(-G)));
      u16* stg = (u16*)(lds + L_CAND);
      stg[lane] = (u16)f2bf(gg * U);
      __builtin_amdgcn_fence(__ATOMIC_RELEASE, "wavefront");
      __builtin_amdgcn_wave_barrier();
      __builtin_amdgcn_fence(__ATOMIC_ACQUIRE, "wavefront");
      if (lane < 8) {
        const u32x4 o = *(const u32x4*)(stg + 8 * lane);
        __builtin_amdgcn_raw_buffer_store_b128(o, rs, WS_GU + (lane * F_ + f0) * 2, 0, AUX_SC1);
      }
      drain();
      if (lane == 0) __hip_atomic_store((gu64*)(ws + WS_F4) + slab, tag, RLX_AGENT);
    }
  } else {
    const int j = b - 216;
    const int c0 = 16 * ((j & 7) * 5 + (j >> 3));
    unsigned key = 0, hv = 0;
    u32x2 stv = {0u, 0u};
    if (wave < 2) {
      const int tk = tid >> 4;
      key = sel[0];
#pragma unroll
      for (int jj = 1; jj < 8; ++jj) key = (tk == jj) ? sel[jj] : key;
      const int idx = 0xffff - (int)(key & 0xffffu);
      hv = (unsigned)__builtin_amdgcn_raw_buffer_load_b16(rs, WS_H + (idx * D_ + c0 + (tid & 15)) * 2, 0, AUX_SC1);
      stv = __builtin_amdgcn_raw_buffer_load_b64(rs, WS_STATS + idx * 8, 0, AUX_SC1);
    }
    if (wave == 0) {
      const gu64* f = (const gu64*)(ws + WS_F4);
      for (int it = 0; it < SPIN_CAP; ++it) {
        bool ok = true;
#pragma unroll
        for (int i = 0; i < 4; ++i) {
          const int fi = lane + 64 * i;
          ok &= __hip_atomic_load(f + (fi < 216 ? fi : 0), RLX_AGENT) == tag;
        }
        if (__all(ok)) break;
        __builtin_amdgcn_s_sleep(1);
      }
    }
    __syncthreads();
    {
      u32x4 af[4];
#pragma unroll
      for (int i = 0; i < 4; ++i) {
        const int ks = wave + 16 * i;
        const int ksc = ks < 54 ? ks : 0;
        af[i] = __builtin_amdgcn_raw_buffer_load_b128(rs, WS_GU + ((tok & 7) * F_ + 32 * ksc + 8 * g) * 2, 0, AUX_SC1);
      }
      f32x4 acc = {0.f, 0.f, 0.f, 0.f};
#pragma unroll
      for (int i = 0; i < 4; ++i) {
        const int ks = wave + 16 * i;
        if (ks < 54) {
          const int k0 = 32 * ks;
          const char* p = lds + L_W5 + (k0 + 8 * g + (i16 >> 2)) * 32 + 8 * (i16 & 3);
          const s16x4 blo = tr_read(p);
          const s16x4 bhi = tr_read(p + 4 * 32);
          const bf16x8 bfrag = __builtin_shufflevector(blo, bhi, 0, 1, 2, 3, 4, 5, 6, 7);
          acc = __builtin_amdgcn_mfma_f32_16x16x32_bf16(__builtin_bit_cast(bf16x8, af[i]), bfrag, acc, 0, 0, 0);
        }
      }
      if (g < 2) {
#pragma unroll
        for (int jj = 0; jj < 4; ++jj) red[(wave * 8 + 4 * g + jj) * 16 + i16] = acc[jj];
      }
    }
    __syncthreads();
    if (wave < 2) {
      const int tk = tid >> 4, col = tid & 15;
      float Y = 0.f;
#pragma unroll
      for (int w = 0; w < 16; ++w) Y += red[(w * 8 + tk) * 16 + col];
      Y = rb(Y);
      const unsigned ord = key >> 16;
      const unsigned rbits = (ord & 0x8000u) ? (ord & 0x7fffu) : (~ord & 0xffffu);
      const float rv = bf2f(rbits);
      const float rsg = rb(1.0f / (1.0f + __expf(-rv)));
      const float y = rb(Y * rsg);
      const float hval = bf2f(hv);
      const float hn = rb(rb(hval - __uint_as_float(stv[0])) / __uint_as_float(stv[1]));
      const float bo = rb(hn + y);
      const int idx = 0xffff - (int)(key & 0xffffu);
      __builtin_amdgcn_raw_buffer_store_b16((short)f2bf(hval + bo), ro, (idx * D_ + c0 + col) * 2, 0, AUX_SC1);
    }
    if (b == 216 && tid == 0) __hip_atomic_store((gu64*)(ws + WS_E), tag, RLX_AGENT);
  }
}

extern "C" void kernel_launch(void* const* d_in, const int* in_sizes, int n_in, void* d_out, int out_size,
                              void* d_ws, size_t ws_size, hipStream_t stream) {
  fused_kernel<<<256, 1024, 0, stream>>>(
      (const u16*)d_in[0], (const u16*)d_in[1], (const u16*)d_in[2], (const u16*)d_in[3], (const u16*)d_in[4],
      (const u16*)d_in[5], (const u16*)d_in[7], (const u16*)d_in[8], (const u16*)d_in[9], (const u16*)d_in[10],
      (u16*)d_out, (char*)d_ws);
}
```

```cpp
#include <hip/hip_runtime.h>
#include <hip/hip_bf16.h>

typedef unsigned short u16;
typedef unsigned long long u64;
using bf16x8 = __attribute__((ext_vector_type(8))) short;
using s16x4 = __attribute__((ext_vector_type(4))) short;
using f32x16 = __attribute__((ext_vector_type(16))) float;
using f32x4 = __attribute__((ext_vector_type(4))) float;
using u32x4 = __attribute__((ext_vector_type(4))) unsigned;
using u32x2 = __attribute__((ext_vector_type(2))) unsigned;
typedef __attribute__((address_space(1))) u64 gu64;

#define DI __device__ __forceinline__
#define RLX_AGENT __ATOMIC_RELAXED, __HIP_MEMORY_SCOPE_AGENT
#define AUX_SC1 16

static constexpr int S_ = 2048;
static constexpr int D_ = 640;
static constexpr int F_ = 1728;

static constexpr unsigned MAT = S_ * D_ * 2;
static constexpr unsigned WS_KVT = 0;
static constexpr unsigned WS_Y2T = MAT;
static constexpr unsigned WS_H = 2 * MAT;
static constexpr unsigned WS_CTL = 3 * MAT;
static constexpr unsigned WS_E = WS_CTL;
static constexpr unsigned WS_F1 = WS_CTL + 2048;
static constexpr unsigned WS_STATS = WS_CTL + 8192;
static constexpr unsigned REP_STRIDE = 4352;
static constexpr int R2 = 16;
static constexpr int RK = 16;
static constexpr unsigned GU_STRIDE = 28160;
static constexpr unsigned WS_F2 = WS_CTL + 32768;
static constexpr unsigned WS_KEYS = WS_F2 + R2 * REP_STRIDE;
static constexpr unsigned WS_GU = WS_KEYS + RK * REP_STRIDE;
static constexpr unsigned WS_BYTES = WS_GU + 8 * GU_STRIDE;

static constexpr int KC_STRIDE = 4160;
static constexpr int KC_BYTES = 8 * KC_STRIDE;
static constexpr int U_ROW = 80;
static constexpr int TEAM = 40960;
static constexpr int L_W4 = 122880;
static constexpr int L_W5 = 81920;
static constexpr int L_TB = 143360;
static constexpr int L_CAND = 154880;
static constexpr int L_RED = 155392;
static constexpr int LDS_BYTES = 163840;

DI float bf2f(unsigned x) { return __uint_as_float(x << 16); }
DI float lo2f(unsigned x) { return __uint_as_float(x << 16); }
DI float hi2f(unsigned x) { return __uint_as_float(x & 0xffff0000u); }
DI unsigned f2bf(float x) {
  __bf16 b = (__bf16)x;
  return (unsigned)__builtin_bit_cast(unsigned short, b);
}
DI float rb(float x) { return (float)((__bf16)x); }
DI unsigned pack2(float lo, float hi) { return f2bf(lo) | (f2bf(hi) << 16); }

template <int CTRL>
DI int dpp_i(int v) { return __builtin_amdgcn_update_dpp(0, v, CTRL, 0xF, 0xF, true); }
DI float wave_sum(float v) {
  v += __int_as_float(dpp_i<0xB1>(__float_as_int(v)));
  v += __int_as_float(dpp_i<0x4E>(__float_as_int(v)));
  v += __int_as_float(dpp_i<0x141>(__float_as_int(v)));
  v += __int_as_float(dpp_i<0x140>(__float_as_int(v)));
  float a = __int_as_float(__builtin_amdgcn_readlane(__float_as_int(v), 0));
  float b = __int_as_float(__builtin_amdgcn_readlane(__float_as_int(v), 16));
  float c = __int_as_float(__builtin_amdgcn_readlane(__float_as_int(v), 32));
  float d = __int_as_float(__builtin_amdgcn_readlane(__float_as_int(v), 48));
  return (a + b) + (c + d);
}
DI unsigned umax(unsigned a, unsigned b) { return a > b ? a : b; }
DI unsigned wave_umax(unsigned v) {
  v = umax(v, (unsigned)dpp_i<0xB1>((int)v));
  v = umax(v, (unsigned)dpp_i<0x4E>((int)v));
  v = umax(v, (unsigned)dpp_i<0x141>((int)v));
  v = umax(v, (unsigned)dpp_i<0x140>((int)v));
  unsigned a = (unsigned)__builtin_amdgcn_readlane((int)v, 0);
  unsigned b = (unsigned)__builtin_amdgcn_readlane((int)v, 16);
  unsigned c = (unsigned)__builtin_amdgcn_readlane((int)v, 32);
  unsigned d = (unsigned)__builtin_amdgcn_readlane((int)v, 48);
  return umax(umax(a, b), umax(c, d));
}
typedef __attribute__((ext_vector_type(4))) short v4i16_t;
DI s16x4 tr_read(const char* p) {
  return __builtin_bit_cast(s16x4, __builtin_amdgcn_ds_read_tr16_b64_v4i16(
      (__attribute__((address_space(3))) v4i16_t*)p));
}
DI void glds16(const void* gsrc, unsigned lds_dst) {
  unsigned keep;
  asm volatile("s_mov_b32 %0, m0\n\ts_mov_b32 m0, %2\n\ts_nop 0\n\tglobal_load_lds_dwordx4 %1, off\n\ts_mov_b32 m0, %0"
               : "=&s"(keep) : "v"(gsrc), "s"(lds_dst) : "memory");
}
DI void drain() { asm volatile("s_waitcnt vmcnt(0)" ::: "memory"); }
static constexpr int SPIN_CAP = 1 << 20;
#define STAMP(i) do {} while (0)

__global__ __launch_bounds__(1024) void fused_kernel(
    const u16* __restrict__ q, const u16* __restrict__ k, const u16* __restrict__ v,
    const u16* __restrict__ shift_k, const u16* __restrict__ ssm_k, const u16* __restrict__ ssm_D,
    const u16* __restrict__ w_r, const u16* __restrict__ w_gate, const u16* __restrict__ w_up,
    const u16* __restrict__ w_down, u16* out, char* ws) {
  __shared__ __attribute__((aligned(16))) char lds[LDS_BYTES];
  const int b = blockIdx.x;
  const int grp = b & 7, ib = b >> 3;
  const int tid = threadIdx.x, lane = tid & 63;
  const int wave = __builtin_amdgcn_readfirstlane(tid >> 6);
  const int team = wave >> 2, tt = tid & 255;
  const __amdgpu_buffer_rsrc_t rs = __builtin_amdgcn_make_buffer_rsrc(ws, 0, WS_BYTES, 0x00020000);
  const __amdgpu_buffer_rsrc_t ro = __builtin_amdgcn_make_buffer_rsrc(out, 0, MAT, 0x00020000);

  STAMP(0);
  u64 tag = 0;
  if (wave == 0) tag = __hip_atomic_load((gu64*)(ws + WS_E), RLX_AGENT) + 1ull;

  const int nch = (ib < 16) ? 3 : 2;
  const int ch = 80 * grp + ((ib < 16) ? 3 * ib : 48 + 2 * (ib - 16)) + team;
  const bool conv_on = team < nch;
  u32x4 klo = {0u, 0u, 0u, 0u}, khi = {0u, 0u, 0u, 0u};
  float Dc = 0.f;
  if (conv_on) {
    const u32x4* kp = (const u32x4*)(ssm_k + (size_t)ch * S_);
    if (tt < 255) klo = kp[254 - tt];
    khi = kp[255 - tt];
    Dc = bf2f(ssm_D[ch]);
  }
  const int t0 = (grp * 32 + ib) * 8;
  unsigned xr[5], wr[5];
  {
    const int t = t0 + (wave & 7);
#pragma unroll
    for (int i = 0; i < 5; ++i) xr[i] = *(const unsigned*)(q + (size_t)t * D_ + 2 * lane + 128 * i);
#pragma unroll
    for (int i = 0; i < 5; ++i) wr[i] = *(const unsigned*)(w_r + 2 * lane + 128 * i);
  }

  if (wave >= 12) {
    const unsigned lbase = (unsigned)(size_t)lds;
    if (b < 216) {
      const int f0 = ((b & 7) * 27 + (b >> 3)) * 8;
#pragma unroll
      for (int i = 0; i < 5; ++i) {
        const int p = tt + 256 * i;
        const u16* src = ((p & 1) ? w_up : w_gate) + (size_t)(p >> 1) * F_ + f0;
        glds16(src, lbase + L_W4 + 16 * ((wave - 12) * 64 + 256 * i));
      }
    } else {
      const int j5 = b - 216;
      const int c05 = 16 * ((j5 & 7) * 5 + (j5 >> 3));
      for (int pb = (wave - 12) * 64; pb < 3456; pb += 256) {
        const int p = pb + lane;
        glds16(w_down + (size_t)(p >> 1) * D_ + c05 + 8 * (p & 1), lbase + L_W5 + 16 * pb);
      }
    }
  }
  {
    const u32x4 ones = {0xffffffffu, 0xffffffffu, 0xffffffffu, 0xffffffffu};
    if (wave == 11 && lane < RK) __builtin_amdgcn_raw_buffer_store_b128(ones, rs, WS_KEYS + lane * REP_STRIDE + t0 * 2, 0, AUX_SC1);
    if (wave == 10 && b < 216)
      __builtin_amdgcn_raw_buffer_store_b128(ones, rs, WS_GU + (lane >> 3) * GU_STRIDE + ((lane & 7) * F_ + ((b & 7) * 27 + (b >> 3)) * 8) * 2, 0, AUX_SC1);
  }

  if (tid < 640) {
    const int tok = tid / 10, chunk = tid - 10 * tok;
    const int t = 64 * ib + tok;
    const int c = 80 * grp + 8 * chunk;
    u32x4 kr[4], sk[4];
#pragma unroll
    for (int i = 0; i < 4; ++i) {
      const int row = t - 3 + i;
      kr[i] = *(const u32x4*)(k + (size_t)(row < 0 ? 0 : row) * D_ + c);
      if (row < 0) kr[i] = (u32x4){0u, 0u, 0u, 0u};
    }
    const u32x4 vr = *(const u32x4*)(v + (size_t)t * D_ + c);
#pragma unroll
    for (int i = 0; i < 4; ++i) sk[i] = *(const u32x4*)(shift_k + (size_t)c * 4 + 8 * i);
#pragma unroll
    for (int j = 0; j < 8; ++j) {
      const unsigned s01 = sk[j >> 1][2 * (j & 1)], s23 = sk[j >> 1][2 * (j & 1) + 1];
      const float w0 = lo2f(s01), w1 = hi2f(s01), w2 = lo2f(s23), w3 = hi2f(s23);
      float kk[4];
#pragma unroll
      for (int i = 0; i < 4; ++i) kk[i] = (j & 1) ? hi2f(kr[i][j >> 1]) : lo2f(kr[i][j >> 1]);
      const float vv = (j & 1) ? hi2f(vr[j >> 1]) : lo2f(vr[j >> 1]);
      float ks = rb(kk[0] * w3);
      ks = rb(ks + rb(kk[1] * w2));
      ks = rb(ks + rb(kk[2] * w1));
      ks = rb(ks + rb(kk[3] * w0));
      *(u16*)(lds + L_TB + (8 * chunk + j) * 144 + 2 * tok) = (u16)f2bf(ks * vv);
    }
  }
  __syncthreads();
  STAMP(1);
  if (tid < 640) {
    const int row = tid >> 3, piece = tid & 7;
    const u32x4 d = *(const u32x4*)(lds + L_TB + row * 144 + 16 * piece);
    __builtin_amdgcn_raw_buffer_store_b128(d, rs, WS_KVT + ((80 * grp + row) * S_ + 64 * ib + 8 * piece) * 2, 0, AUX_SC1);
  }
  char* kc = lds + TEAM * team;
  char* us = kc + KC_BYTES;
  if (conv_on) {
    const u32x4 z4 = {0u, 0u, 0u, 0u};
    if (tt < 160) *(u32x4*)(us + 16 * tt) = z4;
    if (tt < 32) *(u32x4*)(kc + (tt >> 2) * KC_STRIDE + 16 * (256 + (tt & 3))) = z4;
    const unsigned W[8] = {klo[0], klo[1], klo[2], klo[3], khi[0], khi[1], khi[2], khi[3]};
    unsigned E[7], O[7];
#pragma unroll
    for (int d = 0; d < 7; ++d) {
      E[d] = (W[7 - d] >> 16) | (W[7 - d] << 16);
      O[d] = (W[7 - d] & 0xffffu) | (W[6 - d] & 0xffff0000u);
    }
    char* dst = kc + 16 * tt;
#pragma unroll
    for (int qq = 0; qq < 4; ++qq) {
      *(u32x4*)(dst + (7 - 2 * qq) * KC_STRIDE) = (u32x4){E[qq], E[qq + 1], E[qq + 2], E[qq + 3]};
      *(u32x4*)(dst + (6 - 2 * qq) * KC_STRIDE) = (u32x4){O[qq], O[qq + 1], O[qq + 2], O[qq + 3]};
    }
  }
  if (wave < 12) drain();
  __syncthreads();
  if (tid == 0) __hip_atomic_store((gu64*)(ws + WS_F1) + (grp * 32 + ib), tag, RLX_AGENT);
  STAMP(2);

  float xf[10];
#pragma unroll
  for (int i = 0; i < 5; ++i) { xf[2 * i] = lo2f(xr[i]); xf[2 * i + 1] = hi2f(xr[i]); }
  float m1, sd1;
  {
    float s = 0.f;
#pragma unroll
    for (int i = 0; i < 10; ++i) s += xf[i];
    const float mean1 = wave_sum(s) * (1.0f / D_);
    float qq = 0.f;
#pragma unroll
    for (int i = 0; i < 10; ++i) { const float d = xf[i] - mean1; qq += d * d; }
    const float var1 = wave_sum(qq) * (1.0f / D_);
    m1 = rb(mean1);
    sd1 = rb(sqrtf(rb(rb(var1) + 1e-5f)));
  }

  if (wave == 0) {
    const gu64* f = (const gu64*)(ws + WS_F1) + grp * 32 + (lane & 31);
    for (int it = 0; it < SPIN_CAP; ++it) {
      const bool ok = __hip_atomic_load(f, RLX_AGENT) == tag;
      if (__all(ok)) break;
      __builtin_amdgcn_s_sleep(1);
    }
  }
  __syncthreads();
  STAMP(3);
  u32x4 uu = {0u, 0u, 0u, 0u};
  if (conv_on) {
    uu = __builtin_amdgcn_raw_buffer_load_b128(rs, WS_KVT + (ch * S_ + 8 * tt) * 2, 0, AUX_SC1);
    *(u32x4*)(us + ((tt >> 2) + 32) * U_ROW + 16 * (tt & 3)) = uu;
  }
  __syncthreads();
  STAMP(4);
  const int r = lane & 31, hh = lane >> 5;
  f32x16 y0, y1;
#pragma unroll
  for (int i = 0; i < 16; ++i) { y0[i] = 0.f; y1[i] = 0.f; }
  if (conv_on) {
    const int m0 = 8 * (wave & 3);
    const char* pa = kc + (r & 7) * KC_STRIDE + 2 * (2040 - 8 * (r >> 3) + 8 * hh) - 64 * m0;
    const char* pb = us + (r + 32 - m0) * U_ROW + 16 * hh;
#pragma unroll
    for (int mm = 0; mm < 8; ++mm) {
#pragma unroll
      for (int e = 0; e < 2; ++e) {
        const bf16x8 a0 = *(const bf16x8*)(pa - 64 * mm + 32 * e);
        const bf16x8 a1 = *(const bf16x8*)(pa - 64 * mm + 32 * e - 2048);
        const bf16x8 b0 = *(const bf16x8*)(pb - U_ROW * mm + 32 * e);
        const bf16x8 b1 = *(const bf16x8*)(pb - U_ROW * mm + 32 * e + 32 * U_ROW);
        y0 = __builtin_amdgcn_mfma_f32_32x32x16_bf16(a0, b0, y0, 0, 0, 0);
        y1 = __builtin_amdgcn_mfma_f32_32x32x16_bf16(a0, b1, y1, 0, 0, 0);
        y1 = __builtin_amdgcn_mfma_f32_32x32x16_bf16(a1, b0, y1, 0, 0, 0);
      }
    }
  }
  __syncthreads();
  STAMP(5);
  if (conv_on) {
    char* red = kc + (wave & 3) * 9216 + r * 144 + 16 * hh;
#pragma unroll
    for (int qq = 0; qq < 4; ++qq) {
      *(f32x4*)(red + 32 * qq) = (f32x4){y0[4 * qq], y0[4 * qq + 1], y0[4 * qq + 2], y0[4 * qq + 3]};
      *(f32x4*)(red + 32 * 144 + 32 * qq) = (f32x4){y1[4 * qq], y1[4 * qq + 1], y1[4 * qq + 2], y1[4 * qq + 3]};
    }
  }
  __syncthreads();
  if (conv_on) {
    const char* red = kc + (tt >> 2) * 144 + (tt & 3) * 32;
    f32x4 s0 = *(const f32x4*)(red), s1 = *(const f32x4*)(red + 16);
#pragma unroll
    for (int w = 1; w < 4; ++w) {
      s0 += *(const f32x4*)(red + w * 9216);
      s1 += *(const f32x4*)(red + w * 9216 + 16);
    }
    float o[8];
#pragma unroll
    for (int j = 0; j < 8; ++j) {
      const float acc = j < 4 ? s0[j] : s1[j - 4];
      const float kv = (j & 1) ? hi2f(uu[j >> 1]) : lo2f(uu[j >> 1]);
      o[j] = rb(acc) + rb(kv * Dc);
    }
    const u32x4 st = {pack2(o[0], o[1]), pack2(o[2], o[3]), pack2(o[4], o[5]), pack2(o[6], o[7])};
    __builtin_amdgcn_raw_buffer_store_b128(st, rs, WS_Y2T + (ch * S_ + 8 * tt) * 2, 0, AUX_SC1);
  }
  if (wave < 12) drain();
  __syncthreads();
  if (tid < R2) __hip_atomic_store((gu64*)(ws + WS_F2 + tid * REP_STRIDE) + b, tag, RLX_AGENT);
  STAMP(6);

  if (wave == 0) {
    const gu64* f = (const gu64*)(ws + WS_F2 + (b % R2) * REP_STRIDE) + lane;
    for (int it = 0; it < SPIN_CAP; ++it) {
      bool ok = true;
#pragma unroll
      for (int i = 0; i < 4; ++i) ok &= __hip_atomic_load(f + 64 * i, RLX_AGENT) == tag;
      if (__all(ok)) break;
      __builtin_amdgcn_s_sleep(1);
    }
  }
  __syncthreads();
  STAMP(7);
  {
    u16* ys = (u16*)(lds + L_TB);
    if (tid < 640) {
      const u32x4 a = __builtin_amdgcn_raw_buffer_load_b128(rs, WS_Y2T + (tid * S_ + t0) * 2, 0, AUX_SC1);
#pragma unroll
      for (int j = 0; j < 8; ++j) ys[j * 648 + tid] = (u16)((j & 1) ? (a[j >> 1] >> 16) : (a[j >> 1] & 0xffffu));
    }
    __syncthreads();
    STAMP(8);
    if (wave < 8) {
      const int t = t0 + wave;
      float hf[10];
#pragma unroll
      for (int i = 0; i < 5; ++i) {
        const unsigned yy = *(const unsigned*)&ys[wave * 648 + 2 * lane + 128 * i];
        const float ql0 = rb(rb(xf[2 * i] - m1) / sd1);
        const float ql1 = rb(rb(xf[2 * i + 1] - m1) / sd1);
        hf[2 * i] = rb(xf[2 * i] + rb(ql0 * lo2f(yy)));
        hf[2 * i + 1] = rb(xf[2 * i + 1] + rb(ql1 * hi2f(yy)));
      }
      float s = 0.f;
#pragma unroll
      for (int i = 0; i < 10; ++i) s += hf[i];
      const float mean2 = wave_sum(s) * (1.0f / D_);
      float qq = 0.f;
#pragma unroll
      for (int i = 0; i < 10; ++i) { const float d = hf[i] - mean2; qq += d * d; }
      const float var2 = wave_sum(qq) * (1.0f / D_);
      const float m2 = rb(mean2);
      const float sd2 = rb(sqrtf(rb(rb(var2) + 1e-5f)));
      float dot = 0.f;
      unsigned* stg = (unsigned*)(lds + wave * 2560);
#pragma unroll
      for (int i = 0; i < 5; ++i) {
        const float hn0 = rb(rb(hf[2 * i] - m2) / sd2);
        const float hn1 = rb(rb(hf[2 * i + 1] - m2) / sd2);
        dot += hn0 * lo2f(wr[i]) + hn1 * hi2f(wr[i]);
        stg[lane + 64 * i] = pack2(hf[2 * i] + hn0, hf[2 * i + 1] + hn1);
        stg[320 + lane + 64 * i] = pack2(hf[2 * i], hf[2 * i + 1]);
      }
      const float rr = wave_sum(dot);
      __builtin_amdgcn_fence(__ATOMIC_RELEASE, "wavefront");
      __builtin_amdgcn_wave_barrier();
      __builtin_amdgcn_fence(__ATOMIC_ACQUIRE, "wavefront");
      {
        const u32x4 o0 = *(const u32x4*)(stg + 4 * lane);
        const u32x4 h0 = *(const u32x4*)(stg + 320 + 4 * lane);
        __builtin_amdgcn_raw_buffer_store_b128(o0, ro, (t * D_ + 8 * lane) * 2, 0, AUX_SC1);
        __builtin_amdgcn_raw_buffer_store_b128(h0, rs, WS_H + (t * D_ + 8 * lane) * 2, 0, AUX_SC1);
        if (lane < 16) {
          const u32x4 o1 = *(const u32x4*)(stg + 256 + 4 * lane);
          const u32x4 h1 = *(const u32x4*)(stg + 320 + 256 + 4 * lane);
          __builtin_amdgcn_raw_buffer_store_b128(o1, ro, (t * D_ + 512 + 8 * lane) * 2, 0, AUX_SC1);
          __builtin_amdgcn_raw_buffer_store_b128(h1, rs, WS_H + (t * D_ + 512 + 8 * lane) * 2, 0, AUX_SC1);
        }
        if (lane == 0) {
          const u32x2 sv = {__float_as_uint(m2), __float_as_uint(sd2)};
          __builtin_amdgcn_raw_buffer_store_b64(sv, rs, WS_STATS + t * 8, 0, AUX_SC1);
        }
      }
      drain();
      unsigned rbits = f2bf(rr);
      if (rbits == 0xffffu) rbits = 0xfffeu;
      if (lane == 0) ((u16*)(lds + L_CAND))[wave] = (u16)rbits;
    }
    if (wave >= 12) drain();
    __syncthreads();
    if (tid < RK) {
      const u32x4 kq = *(const u32x4*)(lds + L_CAND);
      __builtin_amdgcn_raw_buffer_store_b128(kq, rs, WS_KEYS + tid * REP_STRIDE + t0 * 2, 0, AUX_SC1);
    }
  }

  STAMP(9);
  unsigned* cand = (unsigned*)(lds + L_CAND);
  unsigned kk[8];
  if (wave < 4) {
    u32x4 gq = {0u, 0u, 0u, 0u};
    for (int it = 0; it < SPIN_CAP; ++it) {
      gq = __builtin_amdgcn_raw_buffer_load_b128(rs, WS_KEYS + (b % RK) * REP_STRIDE + tid * 16, 0, AUX_SC1);
      bool ok = true;
#pragma unroll
      for (int d = 0; d < 4; ++d) ok &= ((gq[d] & 0xffffu) != 0xffffu) && ((gq[d] >> 16) != 0xffffu);
      if (__all(ok)) break;
      __builtin_amdgcn_s_sleep(2);
    }
    STAMP(10);
#pragma unroll
    for (int j = 0; j < 8; ++j) {
      unsigned x = (j & 1) ? (gq[j >> 1] >> 16) : (gq[j >> 1] & 0xffffu);
      x = (x & 0x8000u) ? (~x & 0xffffu) : (x | 0x8000u);
      kk[j] = (x << 16) | (unsigned)(0xffff - 8 * tid - j);
    }
    unsigned m = kk[0];
#pragma unroll
    for (int j = 1; j < 8; ++j) m = umax(m, kk[j]);
    const unsigned M1 = wave_umax(m);
    m = 0;
#pragma unroll
    for (int j = 0; j < 8; ++j) m = umax(m, kk[j] == M1 ? 0u : kk[j]);
    const unsigned M2 = wave_umax(m);
    if (lane == 0) { cand[64 + wave] = M2; if (wave == 0) cand[68] = 0; }
  }
  __syncthreads();
  if (wave < 4) {
    const unsigned th = min(min(cand[64], cand[65]), min(cand[66], cand[67]));
#pragma unroll
    for (int j = 0; j < 8; ++j) {
      if (kk[j] >= th) {
        const unsigned pos = atomicAdd(&cand[68], 1u);
        if (pos < 64) cand[pos] = kk[j];
      }
    }
  }
  __syncthreads();
  const unsigned ncand = cand[68];
  if (ncand <= 64) {
    if (wave == 0) {
      const unsigned mine = (lane < (int)ncand) ? cand[lane] : 0u;
      unsigned rank = 0;
      for (unsigned i = 0; i < ncand; ++i) rank += (cand[i] > mine) ? 1u : 0u;
      if (lane < (int)ncand && rank < 8) cand[72 + rank] = mine;
    }
  } else {
    for (int rnd = 0; rnd < 8; ++rnd) {
      __syncthreads();
      if (wave < 4) {
        unsigned m = kk[0];
#pragma unroll
        for (int j = 1; j < 8; ++j) m = umax(m, kk[j]);
        const unsigned M = wave_umax(m);
        if (lane == 0) cand[64 + wave] = M;
      }
      __syncthreads();
      const unsigned M = umax(umax(cand[64], cand[65]), umax(cand[66], cand[67]));
      if (wave < 4) {
#pragma unroll
        for (int j = 0; j < 8; ++j) kk[j] = (kk[j] == M) ? 0u : kk[j];
      }
      if (tid == 0) cand[72 + rnd] = M;
    }
  }
  __syncthreads();
  STAMP(11);
  const int tok = lane & 15, g = lane >> 4, i16 = lane & 15;
  float* red = (float*)(lds + L_RED);
  if (b < 216) {
    const int slab = (b & 7) * 27 + (b >> 3);
    const int f0 = slab * 8;
    if (wave < 10) {
      const unsigned mykey = cand[72 + (tok & 7)];
      const int myidx = 0xffff - (int)(mykey & 0xffffu);
      const u32x2 stv = __builtin_amdgcn_raw_buffer_load_b64(rs, WS_STATS + myidx * 8, 0, AUX_SC1);
      u32x4 hraw[2];
#pragma unroll
      for (int i = 0; i < 2; ++i)
        hraw[i] = __builtin_amdgcn_raw_buffer_load_b128(rs, WS_H + (myidx * D_ + 32 * (2 * wave + i) + 8 * g) * 2, 0, AUX_SC1);
      const float sm = __uint_as_float(stv[0]), ss = __uint_as_float(stv[1]);
      f32x4 acc = {0.f, 0.f, 0.f, 0.f};
#pragma unroll
      for (int i = 0; i < 2; ++i) {
        const int k0 = 32 * (2 * wave + i);
        u32x4 HN;
#pragma unroll
        for (int d = 0; d < 4; ++d) {
          const float a = rb(rb(lo2f(hraw[i][d]) - sm) / ss);
          const float bb = rb(rb(hi2f(hraw[i][d]) - sm) / ss);
          HN[d] = (tok < 8) ? pack2(a, bb) : 0u;
        }
        const char* p = lds + L_W4 + (k0 + 8 * g + (i16 >> 2)) * 32 + 8 * (i16 & 3);
        const s16x4 blo = tr_read(p);
        const s16x4 bhi = tr_read(p + 4 * 32);
        const bf16x8 bfrag = __builtin_shufflevector(blo, bhi, 0, 1, 2, 3, 4, 5, 6, 7);
        acc = __builtin_amdgcn_mfma_f32_16x16x32_bf16(__builtin_bit_cast(bf16x8, HN), bfrag, acc, 0, 0, 0);
      }
      if (g < 2) {
#pragma unroll
        for (int j = 0; j < 4; ++j) red[(wave * 8 + 4 * g + j) * 16 + i16] = acc[j];
      }
    }
    __syncthreads();
    STAMP(12);
    if (wave == 0) {
      const int tk = lane >> 3, col = lane & 7;
      float G = 0.f, U = 0.f;
#pragma unroll
      for (int w = 0; w < 10; ++w) {
        G += red[(w * 8 + tk) * 16 + col];
        U += red[(w * 8 + tk) * 16 + col + 8];
      }
      G = rb(G); U = rb(U);
      const float gg = rb(G / (1.0f + __expf(-G)));
      u16* stg = (u16*)(lds + L_CAND);
      unsigned gub = f2bf(gg * U);
      if (gub == 0xffffu) gub = 0xfffeu;
      stg[lane] = (u16)gub;
      __builtin_amdgcn_fence(__ATOMIC_RELEASE, "wavefront");
      __builtin_amdgcn_wave_barrier();
      __builtin_amdgcn_fence(__ATOMIC_ACQUIRE, "wavefront");
      {
        const u32x4 o = *(const u32x4*)(stg + 8 * (lane & 7));
        __builtin_amdgcn_raw_buffer_store_b128(o, rs, WS_GU + (lane >> 3) * GU_STRIDE + ((lane & 7) * F_ + f0) * 2, 0, AUX_SC1);
      }
      STAMP(13);
    }
  } else {
    const int j = b - 216;
    const int c0 = 16 * ((j & 7) * 5 + (j >> 3));
    unsigned key = 0, hv = 0;
    u32x2 stv = {0u, 0u};
    if (wave < 2) {
      const int tk = tid >> 4;
      key = cand[72 + tk];
      const int idx = 0xffff - (int)(key & 0xffffu);
      hv = (unsigned)__builtin_amdgcn_raw_buffer_load_b16(rs, WS_H + (idx * D_ + c0 + (tid & 15)) * 2, 0, AUX_SC1);
      stv = __builtin_amdgcn_raw_buffer_load_b64(rs, WS_STATS + idx * 8, 0, AUX_SC1);
    }
    {
      u32x4 af[4];
      for (int it = 0; it < SPIN_CAP; ++it) {
        bool ok = true;
#pragma unroll
        for (int i = 0; i < 4; ++i) {
          const int ks = wave + 16 * i;
          const int ksc = ks < 54 ? ks : 0;
          af[i] = __builtin_amdgcn_raw_buffer_load_b128(rs, WS_GU + (j & 7) * GU_STRIDE + ((tok & 7) * F_ + 32 * ksc + 8 * g) * 2, 0, AUX_SC1);
        }
#pragma unroll
        for (int i = 0; i < 4; ++i)
#pragma unroll
          for (int d = 0; d < 4; ++d) ok &= ((af[i][d] & 0xffffu) != 0xffffu) && ((af[i][d] >> 16) != 0xffffu);
        if (__all(ok)) break;
        __builtin_amdgcn_s_sleep(2);
      }
      STAMP(12);
      f32x4 acc = {0.f, 0.f, 0.f, 0.f};
#pragma unroll
      for (int i = 0; i < 4; ++i) {
        const int ks = wave + 16 * i;
        if (ks < 54) {
          const int k0 = 32 * ks;
          const char* p = lds + L_W5 + (k0 + 8 * g + (i16 >> 2)) * 32 + 8 * (i16 & 3);
          const s16x4 blo = tr_read(p);
          const s16x4 bhi = tr_read(p + 4 * 32);
          const bf16x8 bfrag = __builtin_shufflevector(blo, bhi, 0, 1, 2, 3, 4, 5, 6, 7);
          acc = __builtin_amdgcn_mfma_f32_16x16x32_bf16(__builtin_bit_cast(bf16x8, af[i]), bfrag, acc, 0, 0, 0);
        }
      }
      if (g < 2) {
#pragma unroll
        for (int jj = 0; jj < 4; ++jj) red[(wave * 8 + 4 * g + jj) * 16 + i16] = acc[jj];
      }
    }
    __syncthreads();
    if (wave < 2) {
      const int tk = tid >> 4, col = tid & 15;
      float Y = 0.f;
#pragma unroll
      for (int w = 0; w < 16; ++w) Y += red[(w * 8 + tk) * 16 + col];
      Y = rb(Y);
      const unsigned ord = key >> 16;
      const unsigned rbits = (ord & 0x8000u) ? (ord & 0x7fffu) : (~ord & 0xffffu);
      const float rv = bf2f(rbits);
      const float rsg = rb(1.0f / (1.0f + __expf(-rv)));
      const float y = rb(Y * rsg);
      const float hval = bf2f(hv);
      const float hn = rb(rb(hval - __uint_as_float(stv[0])) / __uint_as_float(stv[1]));
      const float bo = rb(hn + y);
      const int idx = 0xffff - (int)(key & 0xffffu);
      __builtin_amdgcn_raw_buffer_store_b16((short)f2bf(hval + bo), ro, (idx * D_ + c0 + col) * 2, 0, AUX_SC1);
    }
    STAMP(13);
    if (b == 216 && tid == 0) __hip_atomic_store((gu64*)(ws + WS_E), tag, RLX_AGENT);
  }
}

extern "C" void kernel_launch(void* const* d_in, const int* in_sizes, int n_in, void* d_out, int out_size,
                              void* d_ws, size_t ws_size, hipStream_t stream) {
  fused_kernel<<<256, 1024, 0, stream>>>(
      (const u16*)d_in[0], (const u16*)d_in[1], (const u16*)d_in[2], (const u16*)d_in[3], (const u16*)d_in[4],
      (const u16*)d_in[5], (const u16*)d_in[7], (const u16*)d_in[8], (const u16*)d_in[9], (const u16*)d_in[10],
      (u16*)d_out, (char*)d_ws);
}
```

```cpp
#include <hip/hip_runtime.h>
#include <hip/hip_bf16.h>

typedef unsigned short u16;
typedef unsigned long long u64;
using bf16x8 = __attribute__((ext_vector_type(8))) short;
using s16x4 = __attribute__((ext_vector_type(4))) short;
using f32x16 = __attribute__((ext_vector_type(16))) float;
using f32x4 = __attribute__((ext_vector_type(4))) float;
using u32x4 = __attribute__((ext_vector_type(4))) unsigned;
using u32x2 = __attribute__((ext_vector_type(2))) unsigned;
typedef __attribute__((address_space(1))) u64 gu64;

#define DI __device__ __forceinline__
#define RLX_AGENT __ATOMIC_RELAXED, __HIP_MEMORY_SCOPE_AGENT
#define AUX_SC1 16

static constexpr int S_ = 2048;
static constexpr int D_ = 640;
static constexpr int F_ = 1728;

static constexpr unsigned MAT = S_ * D_ * 2;
static constexpr unsigned WS_KVT = 0;
static constexpr unsigned WS_Y2T = MAT;
static constexpr unsigned WS_H = 2 * MAT;
static constexpr unsigned WS_CTL = 3 * MAT;
static constexpr unsigned WS_E = WS_CTL;
static constexpr unsigned WS_STATS = WS_CTL + 4096;
static constexpr unsigned REP_STRIDE = 4352;
static constexpr int R1 = 4;
static constexpr int R2 = 16;
static constexpr int RK = 16;
static constexpr int R4 = 8;
static constexpr unsigned GU_STRIDE = 28160;
static constexpr unsigned WS_F1 = WS_CTL + 32768;
static constexpr unsigned WS_F2 = WS_F1 + R1 * REP_STRIDE;
static constexpr unsigned WS_F3 = WS_F2 + R2 * REP_STRIDE;
static constexpr unsigned WS_F4 = WS_F3 + R2 * REP_STRIDE;
static constexpr unsigned WS_KEYS = WS_F4 + R4 * REP_STRIDE;
static constexpr unsigned WS_GU = WS_KEYS + RK * REP_STRIDE;
static constexpr unsigned WS_BYTES = WS_GU + R4 * GU_STRIDE;

static constexpr int KC_STRIDE = 4160;
static constexpr int KC_BYTES = 8 * KC_STRIDE;
static constexpr int U_ROW = 80;
static constexpr int TEAM = 40960;
static constexpr int L_W4 = 122880;
static constexpr int L_W5 = 81920;
static constexpr int L_TB = 143360;
static constexpr int L_CAND = 154880;
static constexpr int L_RED = 155392;
static constexpr int LDS_BYTES = 163840;

DI float bf2f(unsigned x) { return __uint_as_float(x << 16); }
DI float lo2f(unsigned x) { return __uint_as_float(x << 16); }
DI float hi2f(unsigned x) { return __uint_as_float(x & 0xffff0000u); }
DI unsigned f2bf(float x) {
  __bf16 b = (__bf16)x;
  return (unsigned)__builtin_bit_cast(unsigned short, b);
}
DI float rb(float x) { return (float)((__bf16)x); }
DI unsigned pack2(float lo, float hi) { return f2bf(lo) | (f2bf(hi) << 16); }
DI unsigned nosent(unsigned p) {
  if ((p & 0xffffu) == 0xffffu) p ^= 1u;
  if ((p >> 16) == 0xffffu) p ^= 0x10000u;
  return p;
}
DI bool ready4(const u32x4 a) {
  bool ok = true;
#pragma unroll
  for (int d = 0; d < 4; ++d) ok &= ((a[d] & 0xffffu) != 0xffffu) && ((a[d] >> 16) != 0xffffu);
  return ok;
}
DI float fdiv(float a, float b, float rcpb) {
  const float q0 = a * rcpb;
  const float e = __builtin_fmaf(-q0, b, a);
  return __builtin_fmaf(e, rcpb, q0);
}

template <int CTRL>
DI int dpp_i(int v) { return __builtin_amdgcn_update_dpp(0, v, CTRL, 0xF, 0xF, true); }
DI float wave_sum(float v) {
  v += __int_as_float(dpp_i<0xB1>(__float_as_int(v)));
  v += __int_as_float(dpp_i<0x4E>(__float_as_int(v)));
  v += __int_as_float(dpp_i<0x141>(__float_as_int(v)));
  v += __int_as_float(dpp_i<0x140>(__float_as_int(v)));
  float a = __int_as_float(__builtin_amdgcn_readlane(__float_as_int(v), 0));
  float b = __int_as_float(__builtin_amdgcn_readlane(__float_as_int(v), 16));
  float c = __int_as_float(__builtin_amdgcn_readlane(__float_as_int(v), 32));
  float d = __int_as_float(__builtin_amdgcn_readlane(__float_as_int(v), 48));
  return (a + b) + (c + d);
}
DI unsigned umax(unsigned a, unsigned b) { return a > b ? a : b; }
DI unsigned wave_umax(unsigned v) {
  v = umax(v, (unsigned)dpp_i<0xB1>((int)v));
  v = umax(v, (unsigned)dpp_i<0x4E>((int)v));
  v = umax(v, (unsigned)dpp_i<0x141>((int)v));
  v = umax(v, (unsigned)dpp_i<0x140>((int)v));
  unsigned a = (unsigned)__builtin_amdgcn_readlane((int)v, 0);
  unsigned b = (unsigned)__builtin_amdgcn_readlane((int)v, 16);
  unsigned c = (unsigned)__builtin_amdgcn_readlane((int)v, 32);
  unsigned d = (unsigned)__builtin_amdgcn_readlane((int)v, 48);
  return umax(umax(a, b), umax(c, d));
}
typedef __attribute__((ext_vector_type(4))) short v4i16_t;
DI s16x4 tr_read(const char* p) {
  return __builtin_bit_cast(s16x4, __builtin_amdgcn_ds_read_tr16_b64_v4i16(
      (__attribute__((address_space(3))) v4i16_t*)p));
}
DI void glds16(const void* gsrc, unsigned lds_dst) {
  unsigned keep;
  asm volatile("s_mov_b32 %0, m0\n\ts_mov_b32 m0, %2\n\ts_nop 0\n\tglobal_load_lds_dwordx4 %1, off\n\ts_mov_b32 m0, %0"
               : "=&s"(keep) : "v"(gsrc), "s"(lds_dst) : "memory");
}
DI void drain() { asm volatile("s_waitcnt vmcnt(0)" ::: "memory"); }
static constexpr int SPIN_CAP = 1 << 20;
#define STAMP(i) do {} while (0)

__global__ __launch_bounds__(1024) void fused_kernel(
    const u16* __restrict__ q, const u16* __restrict__ k, const u16* __restrict__ v,
    const u16* __restrict__ shift_k, const u16* __restrict__ ssm_k, const u16* __restrict__ ssm_D,
    const u16* __restrict__ w_r, const u16* __restrict__ w_gate, const u16* __restrict__ w_up,
    const u16* __restrict__ w_down, u16* out, char* ws) {
  __shared__ __attribute__((aligned(16))) char lds[LDS_BYTES];
  const int b = blockIdx.x;
  const int grp = b & 7, ib = b >> 3;
  const int tid = threadIdx.x, lane = tid & 63;
  const int wave = __builtin_amdgcn_readfirstlane(tid >> 6);
  const int team = wave >> 2, tt = tid & 255;
  const __amdgpu_buffer_rsrc_t rs = __builtin_amdgcn_make_buffer_rsrc(ws, 0, WS_BYTES, 0x00020000);
  const __amdgpu_buffer_rsrc_t ro = __builtin_amdgcn_make_buffer_rsrc(out, 0, MAT, 0x00020000);
  const u32x4 ones = {0xffffffffu, 0xffffffffu, 0xffffffffu, 0xffffffffu};
  STAMP(0);

  const int p1tok = (tid < 640 ? tid : 0) / 10, p1chunk = (tid < 640 ? tid : 0) - 10 * p1tok;
  const int p1t = 64 * ib + p1tok, p1c = 80 * grp + 8 * p1chunk;
  u32x4 kr[4], sk[4];
#pragma unroll
  for (int i = 0; i < 4; ++i) {
    const int row = p1t - 3 + i;
    kr[i] = *(const u32x4*)(k + (size_t)(row < 0 ? 0 : row) * D_ + p1c);
  }
  const u32x4 vr = *(const u32x4*)(v + (size_t)p1t * D_ + p1c);
#pragma unroll
  for (int i = 0; i < 4; ++i) sk[i] = *(const u32x4*)(shift_k + (size_t)p1c * 4 + 8 * i);
  const int nch = (ib < 16) ? 3 : 2;
  const bool conv_on = team < nch;
  const int ch = 80 * grp + ((ib < 16) ? 3 * ib : 48 + 2 * (ib - 16)) + (conv_on ? team : 0);
  const u32x4* kp = (const u32x4*)(ssm_k + (size_t)ch * S_);
  u32x4 klo = kp[tt < 255 ? 254 - tt : 0];
  const u32x4 khi = kp[255 - tt];
  const unsigned Draw = ssm_D[ch];
  const int t0 = (grp * 32 + ib) * 8;
  unsigned xr[5], wr[5];
  {
    const int t = t0 + (wave & 7);
#pragma unroll
    for (int i = 0; i < 5; ++i) xr[i] = *(const unsigned*)(q + (size_t)t * D_ + 2 * lane + 128 * i);
#pragma unroll
    for (int i = 0; i < 5; ++i) wr[i] = *(const unsigned*)(w_r + 2 * lane + 128 * i);
  }
  u64 eraw = 0;
  if (wave == 0) eraw = __hip_atomic_load((gu64*)(ws + WS_E), RLX_AGENT);

  const unsigned lbase = (unsigned)(size_t)lds;
  if (wave >= 12 && b < 216) {
    const int f0 = ((b & 7) * 27 + (b >> 3)) * 8;
#pragma unroll
    for (int i = 0; i < 5; ++i) {
      const int p = tt + 256 * i;
      const u16* src = ((p & 1) ? w_up : w_gate) + (size_t)(p >> 1) * F_ + f0;
      glds16(src, lbase + L_W4 + 16 * ((wave - 12) * 64 + 256 * i));
    }
  }
  if (wave == 11 && lane < RK) __builtin_amdgcn_raw_buffer_store_b128(ones, rs, WS_KEYS + lane * REP_STRIDE + t0 * 2, 0, AUX_SC1);
  if (wave == 10 && b < 216)
    __builtin_amdgcn_raw_buffer_store_b128(ones, rs, WS_GU + (lane >> 3) * GU_STRIDE + ((b & 7) * 27 + (b >> 3)) * 128 + (lane & 7) * 16, 0, AUX_SC1);

  if (tid < 640) {
#pragma unroll
    for (int i = 0; i < 3; ++i)
      if (p1t - 3 + i < 0) kr[i] = (u32x4){0u, 0u, 0u, 0u};
#pragma unroll
    for (int j = 0; j < 8; ++j) {
      const unsigned s01 = sk[j >> 1][2 * (j & 1)], s23 = sk[j >> 1][2 * (j & 1) + 1];
      const float w0 = lo2f(s01), w1 = hi2f(s01), w2 = lo2f(s23), w3 = hi2f(s23);
      float kk[4];
#pragma unroll
      for (int i = 0; i < 4; ++i) kk[i] = (j & 1) ? hi2f(kr[i][j >> 1]) : lo2f(kr[i][j >> 1]);
      const float vv = (j & 1) ? hi2f(vr[j >> 1]) : lo2f(vr[j >> 1]);
      float ks = rb(kk[0] * w3);
      ks = rb(ks + rb(kk[1] * w2));
      ks = rb(ks + rb(kk[2] * w1));
      ks = rb(ks + rb(kk[3] * w0));
      *(u16*)(lds + L_TB + (8 * p1chunk + j) * 144 + 2 * p1tok) = (u16)f2bf(ks * vv);
    }
  }
  const u64 tag0 = eraw + 1ull;
  if (tid == 0) { *(u64*)(lds + L_CAND + 384) = tag0; *(unsigned*)(lds + L_CAND + 400) = 0u; }
  __syncthreads();
  STAMP(1);
  if (tid < 640) {
    const int row = tid >> 3, piece = tid & 7;
    const u32x4 d = *(const u32x4*)(lds + L_TB + row * 144 + 16 * piece);
    __builtin_amdgcn_raw_buffer_store_b128(d, rs, WS_KVT + ((80 * grp + row) * S_ + 64 * ib + 8 * piece) * 2, 0, AUX_SC1);
  }
  const u64 tag = *(const u64*)(lds + L_CAND + 384);
  char* kc = lds + TEAM * team;
  char* us = kc + KC_BYTES;
  if (conv_on) {
    const u32x4 z4 = {0u, 0u, 0u, 0u};
    if (tt >= 255) klo = z4;
    if (tt < 160) *(u32x4*)(us + 16 * tt) = z4;
    if (tt < 32) *(u32x4*)(kc + (tt >> 2) * KC_STRIDE + 16 * (256 + (tt & 3))) = z4;
    const unsigned W[8] = {klo[0], klo[1], klo[2], klo[3], khi[0], khi[1], khi[2], khi[3]};
    unsigned E[7], O[7];
#pragma unroll
    for (int d = 0; d < 7; ++d) {
      E[d] = (W[7 - d] >> 16) | (W[7 - d] << 16);
      O[d] = (W[7 - d] & 0xffffu) | (W[6 - d] & 0xffff0000u);
    }
    char* dst = kc + 16 * tt;
#pragma unroll
    for (int qq = 0; qq < 4; ++qq) {
      *(u32x4*)(dst + (7 - 2 * qq) * KC_STRIDE) = (u32x4){E[qq], E[qq + 1], E[qq + 2], E[qq + 3]};
      *(u32x4*)(dst + (6 - 2 * qq) * KC_STRIDE) = (u32x4){O[qq], O[qq + 1], O[qq + 2], O[qq + 3]};
    }
  }
  if (wave < 12) drain();
  __syncthreads();
  if (tid < R1) __hip_atomic_store((gu64*)(ws + WS_F1 + tid * REP_STRIDE) + grp * 32 + ib, tag, RLX_AGENT);
  STAMP(2);
  if (wave >= 12 && b >= 216) {
    const int j5 = b - 216;
    const int c05 = 16 * ((j5 & 7) * 5 + (j5 >> 3));
    for (int pb = (wave - 12) * 64; pb < 3456; pb += 256) {
      const int p = pb + lane;
      glds16(w_down + (size_t)(p >> 1) * D_ + c05 + 8 * (p & 1), lbase + L_W5 + 16 * pb);
    }
  }

  float xf[10], ql[10];
#pragma unroll
  for (int i = 0; i < 5; ++i) { xf[2 * i] = lo2f(xr[i]); xf[2 * i + 1] = hi2f(xr[i]); }
  {
    float s = 0.f;
#pragma unroll
    for (int i = 0; i < 10; ++i) s += xf[i];
    const float mean1 = wave_sum(s) * (1.0f / D_);
    float qq = 0.f;
#pragma unroll
    for (int i = 0; i < 10; ++i) { const float d = xf[i] - mean1; qq += d * d; }
    const float var1 = wave_sum(qq) * (1.0f / D_);
    const float m1 = rb(mean1);
    const float sd1 = rb(sqrtf(rb(rb(var1) + 1e-5f)));
    const float rc1 = __builtin_amdgcn_rcpf(sd1);
#pragma unroll
    for (int i = 0; i < 10; ++i) ql[i] = rb(fdiv(rb(xf[i] - m1), sd1, rc1));
    asm volatile("" : "+v"(ql[0]), "+v"(ql[1]), "+v"(ql[2]), "+v"(ql[3]), "+v"(ql[4]), "+v"(ql[5]), "+v"(ql[6]),
                 "+v"(ql[7]), "+v"(ql[8]), "+v"(ql[9]));
  }

  if (wave == 0) {
    const gu64* f = (const gu64*)(ws + WS_F1 + (ib % R1) * REP_STRIDE) + grp * 32 + (lane & 31);
    for (int it = 0; it < SPIN_CAP; ++it) {
      const bool ok = __hip_atomic_load(f, RLX_AGENT) == tag;
      if (__all(ok)) break;
      __builtin_amdgcn_s_sleep(1);
    }
  }
  __syncthreads();
  STAMP(3);
  u32x4 uu = {0u, 0u, 0u, 0u};
  if (conv_on) {
    uu = __builtin_amdgcn_raw_buffer_load_b128(rs, WS_KVT + (ch * S_ + 8 * tt) * 2, 0, AUX_SC1);
    *(u32x4*)(us + ((tt >> 2) + 32) * U_ROW + 16 * (tt & 3)) = uu;
  }
  __syncthreads();
  STAMP(4);
  const int r = lane & 31, hh = lane >> 5;
  f32x16 y0, y1;
#pragma unroll
  for (int i = 0; i < 16; ++i) { y0[i] = 0.f; y1[i] = 0.f; }
  if (conv_on) {
    const int m0 = 8 * (wave & 3);
    const char* pa = kc + (r & 7) * KC_STRIDE + 2 * (2040 - 8 * (r >> 3) + 8 * hh) - 64 * m0;
    const char* pb = us + (r + 32 - m0) * U_ROW + 16 * hh;
#pragma unroll
    for (int mm = 0; mm < 8; ++mm) {
#pragma unroll
      for (int e = 0; e < 2; ++e) {
        const bf16x8 a0 = *(const bf16x8*)(pa - 64 * mm + 32 * e);
        const bf16x8 a1 = *(const bf16x8*)(pa - 64 * mm + 32 * e - 2048);
        const bf16x8 b0 = *(const bf16x8*)(pb - U_ROW * mm + 32 * e);
        const bf16x8 b1 = *(const bf16x8*)(pb - U_ROW * mm + 32 * e + 32 * U_ROW);
        y0 = __builtin_amdgcn_mfma_f32_32x32x16_bf16(a0, b0, y0, 0, 0, 0);
        y1 = __builtin_amdgcn_mfma_f32_32x32x16_bf16(a0, b1, y1, 0, 0, 0);
        y1 = __builtin_amdgcn_mfma_f32_32x32x16_bf16(a1, b0, y1, 0, 0, 0);
      }
    }
  }
  __syncthreads();
  STAMP(5);
  if (conv_on) {
    char* red = kc + (wave & 3) * 9216 + r * 144 + 16 * hh;
#pragma unroll
    for (int qq = 0; qq < 4; ++qq) {
      *(f32x4*)(red + 32 * qq) = (f32x4){y0[4 * qq], y0[4 * qq + 1], y0[4 * qq + 2], y0[4 * qq + 3]};
      *(f32x4*)(red + 32 * 144 + 32 * qq) = (f32x4){y1[4 * qq], y1[4 * qq + 1], y1[4 * qq + 2], y1[4 * qq + 3]};
    }
  }
  __syncthreads();
  if (conv_on) {
    const char* red = kc + (tt >> 2) * 144 + (tt & 3) * 32;
    f32x4 s0 = *(const f32x4*)(red), s1 = *(const f32x4*)(red + 16);
#pragma unroll
    for (int w = 1; w < 4; ++w) {
      s0 += *(const f32x4*)(red + w * 9216);
      s1 += *(const f32x4*)(red + w * 9216 + 16);
    }
    const float Dc = bf2f(Draw);
    float o[8];
#pragma unroll
    for (int j = 0; j < 8; ++j) {
      const float acc = j < 4 ? s0[j] : s1[j - 4];
      const float kv = (j & 1) ? hi2f(uu[j >> 1]) : lo2f(uu[j >> 1]);
      o[j] = rb(acc) + rb(kv * Dc);
    }
    const u32x4 st = {pack2(o[0], o[1]), pack2(o[2], o[3]), pack2(o[4], o[5]), pack2(o[6], o[7])};
    __builtin_amdgcn_raw_buffer_store_b128(st, rs, WS_Y2T + (ch * S_ + 8 * tt) * 2, 0, AUX_SC1);
  }
  if (wave < 12) drain();
  __syncthreads();
  if (tid < R2) __hip_atomic_store((gu64*)(ws + WS_F2 + tid * REP_STRIDE) + b, tag, RLX_AGENT);
  STAMP(6);

  if (wave == 0) {
    const gu64* f = (const gu64*)(ws + WS_F2 + (b % R2) * REP_STRIDE) + lane;
    for (int it = 0; it < SPIN_CAP; ++it) {
      bool ok = true;
#pragma unroll
      for (int i = 0; i < 4; ++i) ok &= __hip_atomic_load(f + 64 * i, RLX_AGENT) == tag;
      if (__all(ok)) break;
      __builtin_amdgcn_s_sleep(1);
    }
  }
  __syncthreads();
  STAMP(7);
  {
    u16* ys = (u16*)(lds + L_TB);
    if (wave < 10) {
      const u32x4 a = __builtin_amdgcn_raw_buffer_load_b128(rs, WS_Y2T + (tid * S_ + t0) * 2, 0, AUX_SC1);
#pragma unroll
      for (int j = 0; j < 8; ++j) ys[j * 648 + tid] = (u16)((j & 1) ? (a[j >> 1] >> 16) : (a[j >> 1] & 0xffffu));
    }
    __syncthreads();
    STAMP(8);
    float hf[10], hn[10];
    float m2 = 0.f, sd2 = 1.f;
    if (wave < 8) {
#pragma unroll
      for (int i = 0; i < 5; ++i) {
        const unsigned yy = *(const unsigned*)&ys[wave * 648 + 2 * lane + 128 * i];
        hf[2 * i] = rb(xf[2 * i] + rb(ql[2 * i] * lo2f(yy)));
        hf[2 * i + 1] = rb(xf[2 * i + 1] + rb(ql[2 * i + 1] * hi2f(yy)));
      }
      float s = 0.f;
#pragma unroll
      for (int i = 0; i < 10; ++i) s += hf[i];
      const float mean2 = wave_sum(s) * (1.0f / D_);
      float qq = 0.f;
#pragma unroll
      for (int i = 0; i < 10; ++i) { const float d = hf[i] - mean2; qq += d * d; }
      const float var2 = wave_sum(qq) * (1.0f / D_);
      m2 = rb(mean2);
      sd2 = rb(sqrtf(rb(rb(var2) + 1e-5f)));
      const float rc2 = __builtin_amdgcn_rcpf(sd2);
      float dot = 0.f;
#pragma unroll
      for (int i = 0; i < 5; ++i) {
        hn[2 * i] = rb(fdiv(rb(hf[2 * i] - m2), sd2, rc2));
        hn[2 * i + 1] = rb(fdiv(rb(hf[2 * i + 1] - m2), sd2, rc2));
        dot += hn[2 * i] * lo2f(wr[i]) + hn[2 * i + 1] * hi2f(wr[i]);
      }
      const float rr = wave_sum(dot);
      unsigned rbits = f2bf(rr);
      if (rbits == 0xffffu) rbits = 0xfffeu;
      if (lane == 0) ((u16*)(lds + L_CAND))[wave] = (u16)rbits;
    }
    __syncthreads();
    if (wave == 8 && lane < RK) {
      const u32x4 kq = *(const u32x4*)(lds + L_CAND);
      __builtin_amdgcn_raw_buffer_store_b128(kq, rs, WS_KEYS + lane * REP_STRIDE + t0 * 2, 0, AUX_SC1);
    }
    if (wave < 8) {
      const int t = t0 + wave;
      unsigned* stg = (unsigned*)(lds + wave * 2560);
#pragma unroll
      for (int i = 0; i < 5; ++i) {
        stg[lane + 64 * i] = pack2(hf[2 * i] + hn[2 * i], hf[2 * i + 1] + hn[2 * i + 1]);
        stg[320 + lane + 64 * i] = pack2(hf[2 * i], hf[2 * i + 1]);
      }
      __builtin_amdgcn_fence(__ATOMIC_RELEASE, "wavefront");
      __builtin_amdgcn_wave_barrier();
      __builtin_amdgcn_fence(__ATOMIC_ACQUIRE, "wavefront");
      const u32x4 o0 = *(const u32x4*)(stg + 4 * lane);
      const u32x4 h0 = *(const u32x4*)(stg + 320 + 4 * lane);
      __builtin_amdgcn_raw_buffer_store_b128(h0, rs, WS_H + (t * D_ + 8 * lane) * 2, 0, AUX_SC1);
      __builtin_amdgcn_raw_buffer_store_b128(o0, ro, (t * D_ + 8 * lane) * 2, 0, AUX_SC1);
      if (lane < 16) {
        const u32x4 o1 = *(const u32x4*)(stg + 256 + 4 * lane);
        const u32x4 h1 = *(const u32x4*)(stg + 320 + 256 + 4 * lane);
        __builtin_amdgcn_raw_buffer_store_b128(h1, rs, WS_H + (t * D_ + 512 + 8 * lane) * 2, 0, AUX_SC1);
        __builtin_amdgcn_raw_buffer_store_b128(o1, ro, (t * D_ + 512 + 8 * lane) * 2, 0, AUX_SC1);
      }
      if (lane == 0) {
        const u32x2 sv = {__float_as_uint(m2), __float_as_uint(sd2)};
        __builtin_amdgcn_raw_buffer_store_b64(sv, rs, WS_STATS + t * 8, 0, AUX_SC1);
      }
    }
    if (wave < 8 || wave >= 12) drain();
    if (wave < 8) {
      unsigned old = 0;
      if (lane == 0) old = atomicAdd((unsigned*)(lds + L_CAND + 400), 1u);
      old = __builtin_amdgcn_readfirstlane(old);
      if (old == 7u && lane < R2) __hip_atomic_store((gu64*)(ws + WS_F3 + lane * REP_STRIDE) + b, tag, RLX_AGENT);
    }
  }
  STAMP(9);

  unsigned* cand = (unsigned*)(lds + L_CAND);
  unsigned kk[8];
  const bool tkw = (wave >= 8 && wave < 12);
  const int tkt = tid - 512;
  if (tkw) {
    u32x4 gq = ones;
    for (int it = 0; it < SPIN_CAP; ++it) {
      gq = __builtin_amdgcn_raw_buffer_load_b128(rs, WS_KEYS + (b % RK) * REP_STRIDE + tkt * 16, 0, AUX_SC1);
      if (__all(ready4(gq))) break;
      __builtin_amdgcn_s_sleep(2);
    }
#pragma unroll
    for (int j = 0; j < 8; ++j) {
      unsigned x = (j & 1) ? (gq[j >> 1] >> 16) : (gq[j >> 1] & 0xffffu);
      x = (x & 0x8000u) ? (~x & 0xffffu) : (x | 0x8000u);
      kk[j] = (x << 16) | (unsigned)(0xffff - 8 * tkt - j);
    }
    unsigned m = kk[0];
#pragma unroll
    for (int j = 1; j < 8; ++j) m = umax(m, kk[j]);
    const unsigned M1 = wave_umax(m);
    m = 0;
#pragma unroll
    for (int j = 0; j < 8; ++j) m = umax(m, kk[j] == M1 ? 0u : kk[j]);
    const unsigned M2 = wave_umax(m);
    if (lane == 0) { cand[64 + wave - 8] = M2; if (wave == 8) cand[68] = 0; }
  }
  __syncthreads();
  STAMP(10);
  if (tkw) {
    const unsigned th = min(min(cand[64], cand[65]), min(cand[66], cand[67]));
#pragma unroll
    for (int j = 0; j < 8; ++j) {
      if (kk[j] >= th) {
        const unsigned pos = atomicAdd(&cand[68], 1u);
        if (pos < 64) cand[pos] = kk[j];
      }
    }
  }
  __syncthreads();
  if (wave == 12) {
    const gu64* f = (const gu64*)(ws + WS_F3 + (b % R2) * REP_STRIDE) + lane;
    for (int it = 0; it < SPIN_CAP; ++it) {
      bool ok = true;
#pragma unroll
      for (int i = 0; i < 4; ++i) ok &= __hip_atomic_load(f + 64 * i, RLX_AGENT) == tag;
      if (__all(ok)) break;
      __builtin_amdgcn_s_sleep(1);
    }
  }
  const unsigned ncand = cand[68];
  if (ncand <= 64) {
    if (wave == 0) {
      const unsigned mine = (lane < (int)ncand) ? cand[lane] : 0u;
      unsigned rank = 0;
      for (unsigned i = 0; i < ncand; ++i) rank += (cand[i] > mine) ? 1u : 0u;
      if (lane < (int)ncand && rank < 8) cand[72 + rank] = mine;
    }
  } else {
    for (int rnd = 0; rnd < 8; ++rnd) {
      __syncthreads();
      if (tkw) {
        unsigned m = kk[0];
#pragma unroll
        for (int j = 1; j < 8; ++j) m = umax(m, kk[j]);
        const unsigned M = wave_umax(m);
        if (lane == 0) cand[64 + wave - 8] = M;
      }
      __syncthreads();
      const unsigned M = umax(umax(cand[64], cand[65]), umax(cand[66], cand[67]));
      if (tkw) {
#pragma unroll
        for (int j = 0; j < 8; ++j) kk[j] = (kk[j] == M) ? 0u : kk[j];
      }
      if (tid == 0) cand[72 + rnd] = M;
    }
  }
  __syncthreads();
  STAMP(11);
  const int tok = lane & 15, g = lane >> 4, i16 = lane & 15;
  float* red = (float*)(lds + L_RED);
  if (b < 216) {
    const int slab = (b & 7) * 27 + (b >> 3);
    const int f0 = slab * 8;
    if (wave < 10) {
      const unsigned mykey = cand[72 + (tok & 7)];
      const int myidx = 0xffff - (int)(mykey & 0xffffu);
      const u32x2 stv = __builtin_amdgcn_raw_buffer_load_b64(rs, WS_STATS + myidx * 8, 0, AUX_SC1);
      u32x4 hraw[2];
#pragma unroll
      for (int i = 0; i < 2; ++i)
        hraw[i] = __builtin_amdgcn_raw_buffer_load_b128(rs, WS_H + (myidx * D_ + 32 * (2 * wave + i) + 8 * g) * 2, 0, AUX_SC1);
      const float sm = __uint_as_float(stv[0]), ss = __uint_as_float(stv[1]);
      const float rcs = __builtin_amdgcn_rcpf(ss);
      f32x4 acc = {0.f, 0.f, 0.f, 0.f};
#pragma unroll
      for (int i = 0; i < 2; ++i) {
        const int k0 = 32 * (2 * wave + i);
        u32x4 HN;
#pragma unroll
        for (int d = 0; d < 4; ++d) {
          const float a = fdiv(rb(lo2f(hraw[i][d]) - sm), ss, rcs);
          const float bb = fdiv(rb(hi2f(hraw[i][d]) - sm), ss, rcs);
          HN[d] = pack2(a, bb);
        }
        const char* p = lds + L_W4 + (k0 + 8 * g + (i16 >> 2)) * 32 + 8 * (i16 & 3);
        const s16x4 blo = tr_read(p);
        const s16x4 bhi = tr_read(p + 4 * 32);
        const bf16x8 bfrag = __builtin_shufflevector(blo, bhi, 0, 1, 2, 3, 4, 5, 6, 7);
        acc = __builtin_amdgcn_mfma_f32_16x16x32_bf16(__builtin_bit_cast(bf16x8, HN), bfrag, acc, 0, 0, 0);
      }
      if (g < 2) {
#pragma unroll
        for (int j = 0; j < 4; ++j) red[(wave * 8 + 4 * g + j) * 16 + i16] = acc[j];
      }
    }
    __syncthreads();
    STAMP(12);
    if (wave == 0) {
      const int tk = lane >> 3, col = lane & 7;
      float G = 0.f, U = 0.f;
#pragma unroll
      for (int w = 0; w < 10; ++w) {
        G += red[(w * 8 + tk) * 16 + col];
        U += red[(w * 8 + tk) * 16 + col + 8];
      }
      G = rb(G); U = rb(U);
      const float gg = rb(G / (1.0f + __expf(-G)));
      u16* stg = (u16*)(lds + L_CAND);
      unsigned gub = f2bf(gg * U);
      if (gub == 0xffffu) gub = 0xfffeu;
      stg[lane] = (u16)gub;
      __builtin_amdgcn_fence(__ATOMIC_RELEASE, "wavefront");
      __builtin_amdgcn_wave_barrier();
      __builtin_amdgcn_fence(__ATOMIC_ACQUIRE, "wavefront");
      {
        const u32x4 o = *(const u32x4*)(stg + 8 * (lane & 7));
        __builtin_amdgcn_raw_buffer_store_b128(o, rs, WS_GU + (lane >> 3) * GU_STRIDE + slab * 128 + (lane & 7) * 16, 0, AUX_SC1);
      }
      if (lane < R4) __hip_atomic_store((gu64*)(ws + WS_F4 + lane * REP_STRIDE) + slab, tag, RLX_AGENT);
      STAMP(13);
    }
  } else {
    const int j = b - 216;
    const int c0 = 16 * ((j & 7) * 5 + (j >> 3));
    unsigned key = 0, hv = 0;
    u32x2 stv = {0u, 0u};
    if (wave < 2) {
      const int tk = tid >> 4;
      key = cand[72 + tk];
      const int idx = 0xffff - (int)(key & 0xffffu);
      hv = (unsigned)(u16)__builtin_amdgcn_raw_buffer_load_b16(rs, WS_H + (idx * D_ + c0 + (tid & 15)) * 2, 0, AUX_SC1);
      stv = __builtin_amdgcn_raw_buffer_load_b64(rs, WS_STATS + idx * 8, 0, AUX_SC1);
    }
    if (wave == 2) {
      const gu64* f = (const gu64*)(ws + WS_F4 + (j % R4) * REP_STRIDE);
      for (int it = 0; it < SPIN_CAP; ++it) {
        bool ok = true;
#pragma unroll
        for (int i = 0; i < 4; ++i) {
          const int fi = lane + 64 * i;
          ok &= __hip_atomic_load(f + (fi < 216 ? fi : 0), RLX_AGENT) == tag;
        }
        if (__all(ok)) break;
        __builtin_amdgcn_s_sleep(1);
      }
    }
    __syncthreads();
    {
      u32x4 af[4];
      for (int it = 0; it < SPIN_CAP; ++it) {
        bool ok = true;
#pragma unroll
        for (int i = 0; i < 4; ++i) {
          const int ks = wave + 16 * i;
          const int ksc = ks < 54 ? ks : 0;
          af[i] = (u32x4){0u, 0u, 0u, 0u};
          if (tok < 8) af[i] = __builtin_amdgcn_raw_buffer_load_b128(rs, WS_GU + (j % R4) * GU_STRIDE + (4 * ksc + g) * 128 + tok * 16, 0, AUX_SC1);
        }
#pragma unroll
        for (int i = 0; i < 4; ++i) ok &= ready4(af[i]);
        if (__all(ok)) break;
        __builtin_amdgcn_s_sleep(2);
      }
      STAMP(12);
      f32x4 acc = {0.f, 0.f, 0.f, 0.f};
#pragma unroll
      for (int i = 0; i < 4; ++i) {
        const int ks = wave + 16 * i;
        if (ks < 54) {
          const int k0 = 32 * ks;
          const char* p = lds + L_W5 + (k0 + 8 * g + (i16 >> 2)) * 32 + 8 * (i16 & 3);
          const s16x4 blo = tr_read(p);
          const s16x4 bhi = tr_read(p + 4 * 32);
          const bf16x8 bfrag = __builtin_shufflevector(blo, bhi, 0, 1, 2, 3, 4, 5, 6, 7);
          acc = __builtin_amdgcn_mfma_f32_16x16x32_bf16(__builtin_bit_cast(bf16x8, af[i]), bfrag, acc, 0, 0, 0);
        }
      }
      if (g < 2) {
#pragma unroll
        for (int jj = 0; jj < 4; ++jj) red[(wave * 8 + 4 * g + jj) * 16 + i16] = acc[jj];
      }
    }
    __syncthreads();
    if (wave < 2) {
      const int tk = tid >> 4, col = tid & 15;
      float Y = 0.f;
#pragma unroll
      for (int w = 0; w < 16; ++w) Y += red[(w * 8 + tk) * 16 + col];
      Y = rb(Y);
      const unsigned ord = key >> 16;
      const unsigned rbits = (ord & 0x8000u) ? (ord & 0x7fffu) : (~ord & 0xffffu);
      const float rv = bf2f(rbits);
      const float rsg = rb(1.0f / (1.0f + __expf(-rv)));
      const float y = rb(Y * rsg);
      const float hval = bf2f(hv);
      const float hn = rb(rb(hval - __uint_as_float(stv[0])) / __uint_as_float(stv[1]));
      const float bo = rb(hn + y);
      const int idx = 0xffff - (int)(key & 0xffffu);
      __builtin_amdgcn_raw_buffer_store_b16((short)f2bf(hval + bo), ro, (idx * D_ + c0 + col) * 2, 0, AUX_SC1);
    }
    STAMP(13);
    if (b == 216 && tid == 0) __hip_atomic_store((gu64*)(ws + WS_E), tag, RLX_AGENT);
  }
}

extern "C" void kernel_launch(void* const* d_in, const int* in_sizes, int n_in, void* d_out, int out_size,
                              void* d_ws, size_t ws_size, hipStream_t stream) {
  fused_kernel<<<256, 1024, 0, stream>>>(
      (const u16*)d_in[0], (const u16*)d_in[1], (const u16*)d_in[2], (const u16*)d_in[3], (const u16*)d_in[4],
      (const u16*)d_in[5], (const u16*)d_in[7], (const u16*)d_in[8], (const u16*)d_in[9], (const u16*)d_in[10],
      (u16*)d_out, (char*)d_ws);
}
```

```cpp
#include <hip/hip_runtime.h>
#include <hip/hip_bf16.h>

typedef unsigned short u16;
typedef unsigned long long u64;
using bf16x8 = __attribute__((ext_vector_type(8))) short;
using s16x4 = __attribute__((ext_vector_type(4))) short;
using f32x16 = __attribute__((ext_vector_type(16))) float;
using f32x4 = __attribute__((ext_vector_type(4))) float;
using u32x4 = __attribute__((ext_vector_type(4))) unsigned;
using u32x2 = __attribute__((ext_vector_type(2))) unsigned;
typedef __attribute__((address_space(1))) u64 gu64;

#define DI __device__ __forceinline__
#define RLX_AGENT __ATOMIC_RELAXED, __HIP_MEMORY_SCOPE_AGENT
#define AUX_SC1 16

static constexpr int S_ = 2048;
static constexpr int D_ = 640;
static constexpr int F_ = 1728;

static constexpr unsigned MAT = S_ * D_ * 2;
static constexpr unsigned WS_KVT = 0;
static constexpr unsigned WS_Y2T = MAT;
static constexpr unsigned WS_H = 2 * MAT;
static constexpr unsigned WS_CTL = 3 * MAT;
static constexpr unsigned WS_E = WS_CTL;
static constexpr unsigned WS_STATS = WS_CTL + 4096;
static constexpr unsigned REP_STRIDE = 4352;
static constexpr int R1 = 4;
static constexpr int R2 = 16;
static constexpr int RK = 16;
static constexpr int R4 = 8;
static constexpr unsigned GU_STRIDE = 28160;
static constexpr unsigned WS_F1 = WS_CTL + 32768;
static constexpr unsigned WS_F2 = WS_F1 + R1 * REP_STRIDE;
static constexpr unsigned WS_F3 = WS_F2 + R2 * REP_STRIDE;
static constexpr unsigned WS_F4 = WS_F3 + R2 * REP_STRIDE;
static constexpr unsigned WS_KEYS = WS_F4 + R4 * REP_STRIDE;
static constexpr unsigned WS_GU = WS_KEYS + RK * REP_STRIDE;
static constexpr unsigned WS_BYTES = WS_GU + R4 * GU_STRIDE;

static constexpr int KC_STRIDE = 4160;
static constexpr int KC_BYTES = 8 * KC_STRIDE;
static constexpr int U_ROW = 80;
static constexpr int TEAM = 40960;
static constexpr int L_W4 = 122880;
static constexpr int L_W5 = 81920;
static constexpr int L_TB = 143360;
static constexpr int L_CAND = 154880;
static constexpr int L_RED = 155392;
static constexpr int LDS_BYTES = 163840;

DI float bf2f(unsigned x) { return __uint_as_float(x << 16); }
DI float lo2f(unsigned x) { return __uint_as_float(x << 16); }
DI float hi2f(unsigned x) { return __uint_as_float(x & 0xffff0000u); }
DI unsigned f2bf(float x) {
  __bf16 b = (__bf16)x;
  return (unsigned)__builtin_bit_cast(unsigned short, b);
}
DI float rb(float x) { return (float)((__bf16)x); }
DI unsigned pack2(float lo, float hi) { return f2bf(lo) | (f2bf(hi) << 16); }
DI unsigned nosent(unsigned p) {
  if ((p & 0xffffu) == 0xffffu) p ^= 1u;
  if ((p >> 16) == 0xffffu) p ^= 0x10000u;
  return p;
}
DI bool ready4(const u32x4 a) {
  bool ok = true;
#pragma unroll
  for (int d = 0; d < 4; ++d) ok &= ((a[d] & 0xffffu) != 0xffffu) && ((a[d] >> 16) != 0xffffu);
  return ok;
}
DI float fdiv(float a, float b, float rcpb) {
  const float q0 = a * rcpb;
  const float e = __builtin_fmaf(-q0, b, a);
  return __builtin_fmaf(e, rcpb, q0);
}

template <int CTRL>
DI int dpp_i(int v) { return __builtin_amdgcn_update_dpp(0, v, CTRL, 0xF, 0xF, true); }
DI float wave_sum(float v) {
  v += __int_as_float(dpp_i<0xB1>(__float_as_int(v)));
  v += __int_as_float(dpp_i<0x4E>(__float_as_int(v)));
  v += __int_as_float(dpp_i<0x141>(__float_as_int(v)));
  v += __int_as_float(dpp_i<0x140>(__float_as_int(v)));
  float a = __int_as_float(__builtin_amdgcn_readlane(__float_as_int(v), 0));
  float b = __int_as_float(__builtin_amdgcn_readlane(__float_as_int(v), 16));
  float c = __int_as_float(__builtin_amdgcn_readlane(__float_as_int(v), 32));
  float d = __int_as_float(__builtin_amdgcn_readlane(__float_as_int(v), 48));
  return (a + b) + (c + d);
}
DI unsigned umax(unsigned a, unsigned b) { return a > b ? a : b; }
DI unsigned wave_umax(unsigned v) {
  v = umax(v, (unsigned)dpp_i<0xB1>((int)v));
  v = umax(v, (unsigned)dpp_i<0x4E>((int)v));
  v = umax(v, (unsigned)dpp_i<0x141>((int)v));
  v = umax(v, (unsigned)dpp_i<0x140>((int)v));
  unsigned a = (unsigned)__builtin_amdgcn_readlane((int)v, 0);
  unsigned b = (unsigned)__builtin_amdgcn_readlane((int)v, 16);
  unsigned c = (unsigned)__builtin_amdgcn_readlane((int)v, 32);
  unsigned d = (unsigned)__builtin_amdgcn_readlane((int)v, 48);
  return umax(umax(a, b), umax(c, d));
}
typedef __attribute__((ext_vector_type(4))) short v4i16_t;
DI s16x4 tr_read(const char* p) {
  return __builtin_bit_cast(s16x4, __builtin_amdgcn_ds_read_tr16_b64_v4i16(
      (__attribute__((address_space(3))) v4i16_t*)p));
}
DI void glds16(const void* gsrc, unsigned lds_dst) {
  unsigned keep;
  asm volatile("s_mov_b32 %0, m0\n\ts_mov_b32 m0, %2\n\ts_nop 0\n\tglobal_load_lds_dwordx4 %1, off\n\ts_mov_b32 m0, %0"
               : "=&s"(keep) : "v"(gsrc), "s"(lds_dst) : "memory");
}
DI void drain() { asm volatile("s_waitcnt vmcnt(0)" ::: "memory"); }
static constexpr int SPIN_CAP = 1 << 20;
#define STAMP(i) do {} while (0)

__global__ __launch_bounds__(1024) void fused_kernel(
    const u16* __restrict__ q, const u16* __restrict__ k, const u16* __restrict__ v,
    const u16* __restrict__ shift_k, const u16* __restrict__ ssm_k, const u16* __restrict__ ssm_D,
    const u16* __restrict__ w_r, const u16* __restrict__ w_gate, const u16* __restrict__ w_up,
    const u16* __restrict__ w_down, u16* out, char* ws) {
  __shared__ __attribute__((aligned(16))) char lds[LDS_BYTES];
  const int b = blockIdx.x;
  const int grp = b & 7, ib = b >> 3;
  const int tid = threadIdx.x, lane = tid & 63;
  const int wave = __builtin_amdgcn_readfirstlane(tid >> 6);
  const int team = wave >> 2, tt = tid & 255;
  const __amdgpu_buffer_rsrc_t rs = __builtin_amdgcn_make_buffer_rsrc(ws, 0, WS_BYTES, 0x00020000);
  const __amdgpu_buffer_rsrc_t ro = __builtin_amdgcn_make_buffer_rsrc(out, 0, MAT, 0x00020000);
  const u32x4 ones = {0xffffffffu, 0xffffffffu, 0xffffffffu, 0xffffffffu};
  STAMP(0);

  const int p1tok = (tid < 640 ? tid : 0) / 10, p1chunk = (tid < 640 ? tid : 0) - 10 * p1tok;
  const int p1t = 64 * ib + p1tok, p1c = 80 * grp + 8 * p1chunk;
  u32x4 kr[4], sk[4];
#pragma unroll
  for (int i = 0; i < 4; ++i) {
    const int row = p1t - 3 + i;
    kr[i] = *(const u32x4*)(k + (size_t)(row < 0 ? 0 : row) * D_ + p1c);
  }
  const u32x4 vr = *(const u32x4*)(v + (size_t)p1t * D_ + p1c);
#pragma unroll
  for (int i = 0; i < 4; ++i) sk[i] = *(const u32x4*)(shift_k + (size_t)p1c * 4 + 8 * i);
  const int nch = (ib < 16) ? 3 : 2;
  const bool conv_on = team < nch;
  const int ch = 80 * grp + ((ib < 16) ? 3 * ib : 48 + 2 * (ib - 16)) + (conv_on ? team : 0);
  const u32x4* kp = (const u32x4*)(ssm_k + (size_t)ch * S_);
  u32x4 klo = kp[tt < 255 ? 254 - tt : 0];
  const u32x4 khi = kp[255 - tt];
  const unsigned Draw = ssm_D[ch];
  const int t0 = (grp * 32 + ib) * 8;
  unsigned xr[5], wr[5];
  {
    const int t = t0 + (wave & 7);
#pragma unroll
    for (int i = 0; i < 5; ++i) xr[i] = *(const unsigned*)(q + (size_t)t * D_ + 2 * lane + 128 * i);
#pragma unroll
    for (int i = 0; i < 5; ++i) wr[i] = *(const unsigned*)(w_r + 2 * lane + 128 * i);
  }
  u64 eraw = 0;
  if (wave == 0) eraw = __hip_atomic_load((gu64*)(ws + WS_E), RLX_AGENT);

  const unsigned lbase = (unsigned)(size_t)lds;
  if (wave >= 12 && b < 216) {
    const int f0 = ((b & 7) * 27 + (b >> 3)) * 8;
#pragma unroll
    for (int i = 0; i < 5; ++i) {
      const int p = tt + 256 * i;
      const u16* src = ((p & 1) ? w_up : w_gate) + (size_t)(p >> 1) * F_ + f0;
      glds16(src, lbase + L_W4 + 16 * ((wave - 12) * 64 + 256 * i));
    }
  }
  if (wave == 11 && lane < RK) __builtin_amdgcn_raw_buffer_store_b128(ones, rs, WS_KEYS + lane * REP_STRIDE + t0 * 2, 0, AUX_SC1);
  if (wave == 10 && b < 216)
    __builtin_amdgcn_raw_buffer_store_b128(ones, rs, WS_GU + (lane >> 3) * GU_STRIDE + ((b & 7) * 27 + (b >> 3)) * 128 + (lane & 7) * 16, 0, AUX_SC1);

  if (tid < 640) {
#pragma unroll
    for (int i = 0; i < 3; ++i)
      if (p1t - 3 + i < 0) kr[i] = (u32x4){0u, 0u, 0u, 0u};
#pragma unroll
    for (int j = 0; j < 8; ++j) {
      const unsigned s01 = sk[j >> 1][2 * (j & 1)], s23 = sk[j >> 1][2 * (j & 1) + 1];
      const float w0 = lo2f(s01), w1 = hi2f(s01), w2 = lo2f(s23), w3 = hi2f(s23);
      float kk[4];
#pragma unroll
      for (int i = 0; i < 4; ++i) kk[i] = (j & 1) ? hi2f(kr[i][j >> 1]) : lo2f(kr[i][j >> 1]);
      const float vv = (j & 1) ? hi2f(vr[j >> 1]) : lo2f(vr[j >> 1]);
      float ks = rb(kk[0] * w3);
      ks = rb(ks + rb(kk[1] * w2));
      ks = rb(ks + rb(kk[2] * w1));
      ks = rb(ks + rb(kk[3] * w0));
      *(u16*)(lds + L_TB + (8 * p1chunk + j) * 144 + 2 * p1tok) = (u16)f2bf(ks * vv);
    }
  }
  const u64 tag0 = eraw + 1ull;
  if (tid == 0) { *(u64*)(lds + L_CAND + 384) = tag0; *(unsigned*)(lds + L_CAND + 400) = 0u; }
  __syncthreads();
  STAMP(1);
  if (tid < 640) {
    const int row = tid >> 3, piece = tid & 7;
    const u32x4 d = *(const u32x4*)(lds + L_TB + row * 144 + 16 * piece);
    __builtin_amdgcn_raw_buffer_store_b128(d, rs, WS_KVT + ((80 * grp + row) * S_ + 64 * ib + 8 * piece) * 2, 0, AUX_SC1);
  }
  const u64 tag = *(const u64*)(lds + L_CAND + 384);
  char* kc = lds + TEAM * team;
  char* us = kc + KC_BYTES;
  if (conv_on) {
    const u32x4 z4 = {0u, 0u, 0u, 0u};
    if (tt >= 255) klo = z4;
    if (tt < 160) *(u32x4*)(us + 16 * tt) = z4;
    if (tt < 32) *(u32x4*)(kc + (tt >> 2) * KC_STRIDE + 16 * (256 + (tt & 3))) = z4;
    const unsigned W[8] = {klo[0], klo[1], klo[2], klo[3], khi[0], khi[1], khi[2], khi[3]};
    unsigned E[7], O[7];
#pragma unroll
    for (int d = 0; d < 7; ++d) {
      E[d] = (W[7 - d] >> 16) | (W[7 - d] << 16);
      O[d] = (W[7 - d] & 0xffffu) | (W[6 - d] & 0xffff0000u);
    }
    char* dst = kc + 16 * tt;
#pragma unroll
    for (int qq = 0; qq < 4; ++qq) {
      *(u32x4*)(dst + (7 - 2 * qq) * KC_STRIDE) = (u32x4){E[qq], E[qq + 1], E[qq + 2], E[qq + 3]};
      *(u32x4*)(dst + (6 - 2 * qq) * KC_STRIDE) = (u32x4){O[qq], O[qq + 1], O[qq + 2], O[qq + 3]};
    }
  }
  if (wave < 12) drain();
  __syncthreads();
  if (tid < R1) __hip_atomic_store((gu64*)(ws + WS_F1 + tid * REP_STRIDE) + grp * 32 + ib, tag, RLX_AGENT);
  STAMP(2);
  if (wave >= 12 && b >= 216) {
    const int j5 = b - 216;
    const int c05 = 16 * ((j5 & 7) * 5 + (j5 >> 3));
    for (int pb = (wave - 12) * 64; pb < 3456; pb += 256) {
      const int p = pb + lane;
      glds16(w_down + (size_t)(p >> 1) * D_ + c05 + 8 * (p & 1), lbase + L_W5 + 16 * pb);
    }
  }

  float xf[10], ql[10];
#pragma unroll
  for (int i = 0; i < 5; ++i) { xf[2 * i] = lo2f(xr[i]); xf[2 * i + 1] = hi2f(xr[i]); }
  {
    float s = 0.f;
#pragma unroll
    for (int i = 0; i < 10; ++i) s += xf[i];
    const float mean1 = wave_sum(s) * (1.0f / D_);
    float qq = 0.f;
#pragma unroll
    for (int i = 0; i < 10; ++i) { const float d = xf[i] - mean1; qq += d * d; }
    const float var1 = wave_sum(qq) * (1.0f / D_);
    const float m1 = rb(mean1);
    const float sd1 = rb(sqrtf(rb(rb(var1) + 1e-5f)));
    const float rc1 = __builtin_amdgcn_rcpf(sd1);
#pragma unroll
    for (int i = 0; i < 10; ++i) ql[i] = rb(fdiv(rb(xf[i] - m1), sd1, rc1));
    asm volatile("" : "+v"(ql[0]), "+v"(ql[1]), "+v"(ql[2]), "+v"(ql[3]), "+v"(ql[4]), "+v"(ql[5]), "+v"(ql[6]),
                 "+v"(ql[7]), "+v"(ql[8]), "+v"(ql[9]));
  }

  if (wave == 0) {
    const gu64* f = (const gu64*)(ws + WS_F1 + (ib % R1) * REP_STRIDE) + grp * 32 + (lane & 31);
    for (int it = 0; it < SPIN_CAP; ++it) {
      const bool ok = __hip_atomic_load(f, RLX_AGENT) == tag;
      if (__all(ok)) break;
      __builtin_amdgcn_s_sleep(1);
    }
  }
  __syncthreads();
  STAMP(3);
  u32x4 uu = {0u, 0u, 0u, 0u};
  if (conv_on) {
    uu = __builtin_amdgcn_raw_buffer_load_b128(rs, WS_KVT + (ch * S_ + 8 * tt) * 2, 0, AUX_SC1);
    *(u32x4*)(us + ((tt >> 2) + 32) * U_ROW + 16 * (tt & 3)) = uu;
  }
  __syncthreads();
  STAMP(4);
  const int r = lane & 31, hh = lane >> 5;
  f32x16 y0, y1;
#pragma unroll
  for (int i = 0; i < 16; ++i) { y0[i] = 0.f; y1[i] = 0.f; }
  if (conv_on) {
    const int m0 = 8 * (wave & 3);
    const char* pa = kc + (r & 7) * KC_STRIDE + 2 * (2040 - 8 * (r >> 3) + 8 * hh) - 64 * m0;
    const char* pb = us + (r + 32 - m0) * U_ROW + 16 * hh;
#pragma unroll
    for (int mm = 0; mm < 8; ++mm) {
#pragma unroll
      for (int e = 0; e < 2; ++e) {
        const bf16x8 a0 = *(const bf16x8*)(pa - 64 * mm + 32 * e);
        const bf16x8 a1 = *(const bf16x8*)(pa - 64 * mm + 32 * e - 2048);
        const bf16x8 b0 = *(const bf16x8*)(pb - U_ROW * mm + 32 * e);
        const bf16x8 b1 = *(const bf16x8*)(pb - U_ROW * mm + 32 * e + 32 * U_ROW);
        y0 = __builtin_amdgcn_mfma_f32_32x32x16_bf16(a0, b0, y0, 0, 0, 0);
        y1 = __builtin_amdgcn_mfma_f32_32x32x16_bf16(a0, b1, y1, 0, 0, 0);
        y1 = __builtin_amdgcn_mfma_f32_32x32x16_bf16(a1, b0, y1, 0, 0, 0);
      }
    }
  }
  __syncthreads();
  STAMP(5);
  if (conv_on) {
    char* red = kc + (wave & 3) * 9216 + r * 144 + 16 * hh;
#pragma unroll
    for (int qq = 0; qq < 4; ++qq) {
      *(f32x4*)(red + 32 * qq) = (f32x4){y0[4 * qq], y0[4 * qq + 1], y0[4 * qq + 2], y0[4 * qq + 3]};
      *(f32x4*)(red + 32 * 144 + 32 * qq) = (f32x4){y1[4 * qq], y1[4 * qq + 1], y1[4 * qq + 2], y1[4 * qq + 3]};
    }
  }
  __syncthreads();
  if (conv_on) {
    const char* red = kc + (tt >> 2) * 144 + (tt & 3) * 32;
    f32x4 s0 = *(const f32x4*)(red), s1 = *(const f32x4*)(red + 16);
#pragma unroll
    for (int w = 1; w < 4; ++w) {
      s0 += *(const f32x4*)(red + w * 9216);
      s1 += *(const f32x4*)(red + w * 9216 + 16);
    }
    const float Dc = bf2f(Draw);
    float o[8];
#pragma unroll
    for (int j = 0; j < 8; ++j) {
      const float acc = j < 4 ? s0[j] : s1[j - 4];
      const float kv = (j & 1) ? hi2f(uu[j >> 1]) : lo2f(uu[j >> 1]);
      o[j] = rb(acc) + rb(kv * Dc);
    }
    const u32x4 st = {pack2(o[0], o[1]), pack2(o[2], o[3]), pack2(o[4], o[5]), pack2(o[6], o[7])};
    __builtin_amdgcn_raw_buffer_store_b128(st, rs, WS_Y2T + (ch * S_ + 8 * tt) * 2, 0, AUX_SC1);
  }
  if (wave < 12) drain();
  __syncthreads();
  if (tid < R2) __hip_atomic_store((gu64*)(ws + WS_F2 + tid * REP_STRIDE) + b, tag, RLX_AGENT);
  STAMP(6);

  if (wave == 0) {
    const gu64* f = (const gu64*)(ws + WS_F2 + (b % R2) * REP_STRIDE) + lane;
    for (int it = 0; it < SPIN_CAP; ++it) {
      bool ok = true;
#pragma unroll
      for (int i = 0; i < 4; ++i) ok &= __hip_atomic_load(f + 64 * i, RLX_AGENT) == tag;
      if (__all(ok)) break;
      __builtin_amdgcn_s_sleep(1);
    }
  }
  __syncthreads();
  STAMP(7);
  {
    u16* ys = (u16*)(lds + L_TB);
    if (wave < 10) {
      const u32x4 a = __builtin_amdgcn_raw_buffer_load_b128(rs, WS_Y2T + (tid * S_ + t0) * 2, 0, AUX_SC1);
#pragma unroll
      for (int j = 0; j < 8; ++j) ys[j * 648 + tid] = (u16)((j & 1) ? (a[j >> 1] >> 16) : (a[j >> 1] & 0xffffu));
    }
    __syncthreads();
    STAMP(8);
    float hf[10], hn[10];
    float m2 = 0.f, sd2 = 1.f;
    if (wave < 8) {
#pragma unroll
      for (int i = 0; i < 5; ++i) {
        const unsigned yy = *(const unsigned*)&ys[wave * 648 + 2 * lane + 128 * i];
        hf[2 * i] = rb(xf[2 * i] + rb(ql[2 * i] * lo2f(yy)));
        hf[2 * i + 1] = rb(xf[2 * i + 1] + rb(ql[2 * i + 1] * hi2f(yy)));
      }
      float s = 0.f;
#pragma unroll
      for (int i = 0; i < 10; ++i) s += hf[i];
      const float mean2 = wave_sum(s) * (1.0f / D_);
      float qq = 0.f;
#pragma unroll
      for (int i = 0; i < 10; ++i) { const float d = hf[i] - mean2; qq += d * d; }
      const float var2 = wave_sum(qq) * (1.0f / D_);
      m2 = rb(mean2);
      sd2 = rb(sqrtf(rb(rb(var2) + 1e-5f)));
      const float rc2 = __builtin_amdgcn_rcpf(sd2);
      float dot = 0.f;
#pragma unroll
      for (int i = 0; i < 5; ++i) {
        hn[2 * i] = rb(fdiv(rb(hf[2 * i] - m2), sd2, rc2));
        hn[2 * i + 1] = rb(fdiv(rb(hf[2 * i + 1] - m2), sd2, rc2));
        dot += hn[2 * i] * lo2f(wr[i]) + hn[2 * i + 1] * hi2f(wr[i]);
      }
      const float rr = wave_sum(dot);
      unsigned rbits = f2bf(rr);
      if (rbits == 0xffffu) rbits = 0xfffeu;
      if (lane == 0) ((u16*)(lds + L_CAND))[wave] = (u16)rbits;
    }
    __syncthreads();
    if (wave == 8 && lane < RK) {
      const u32x4 kq = *(const u32x4*)(lds + L_CAND);
      __builtin_amdgcn_raw_buffer_store_b128(kq, rs, WS_KEYS + lane * REP_STRIDE + t0 * 2, 0, AUX_SC1);
    }
    if (wave < 8) {
      const int t = t0 + wave;
      unsigned* stg = (unsigned*)(lds + wave * 2560);
#pragma unroll
      for (int i = 0; i < 5; ++i) {
        stg[lane + 64 * i] = pack2(hf[2 * i] + hn[2 * i], hf[2 * i + 1] + hn[2 * i + 1]);
        stg[320 + lane + 64 * i] = pack2(hf[2 * i], hf[2 * i + 1]);
      }
      __builtin_amdgcn_fence(__ATOMIC_RELEASE, "wavefront");
      __builtin_amdgcn_wave_barrier();
      __builtin_amdgcn_fence(__ATOMIC_ACQUIRE, "wavefront");
      const u32x4 h0 = *(const u32x4*)(stg + 320 + 4 * lane);
      __builtin_amdgcn_raw_buffer_store_b128(h0, rs, WS_H + (t * D_ + 8 * lane) * 2, 0, AUX_SC1);
      if (lane < 16) {
        const u32x4 h1 = *(const u32x4*)(stg + 320 + 256 + 4 * lane);
        __builtin_amdgcn_raw_buffer_store_b128(h1, rs, WS_H + (t * D_ + 512 + 8 * lane) * 2, 0, AUX_SC1);
      }
      if (lane == 0) {
        const u32x2 sv = {__float_as_uint(m2), __float_as_uint(sd2)};
        __builtin_amdgcn_raw_buffer_store_b64(sv, rs, WS_STATS + t * 8, 0, AUX_SC1);
      }
    }
    if (wave < 8 || wave >= 12) drain();
    if (wave < 8) {
      unsigned old = 0;
      if (lane == 0) old = atomicAdd((unsigned*)(lds + L_CAND + 400), 1u);
      old = __builtin_amdgcn_readfirstlane(old);
      if (old == 7u && lane < R2) __hip_atomic_store((gu64*)(ws + WS_F3 + lane * REP_STRIDE) + b, tag, RLX_AGENT);
    }
  }
  STAMP(9);

  unsigned* cand = (unsigned*)(lds + L_CAND);
  unsigned kk[8];
  const bool tkw = (wave >= 8 && wave < 12);
  const int tkt = tid - 512;
  if (tkw) {
    u32x4 gq = ones;
    __builtin_amdgcn_s_sleep(24);
    for (int it = 0; it < SPIN_CAP; ++it) {
      gq = __builtin_amdgcn_raw_buffer_load_b128(rs, WS_KEYS + (b % RK) * REP_STRIDE + tkt * 16, 0, AUX_SC1);
      if (__all(ready4(gq))) break;
      __builtin_amdgcn_s_sleep(10);
    }
#pragma unroll
    for (int j = 0; j < 8; ++j) {
      unsigned x = (j & 1) ? (gq[j >> 1] >> 16) : (gq[j >> 1] & 0xffffu);
      x = (x & 0x8000u) ? (~x & 0xffffu) : (x | 0x8000u);
      kk[j] = (x << 16) | (unsigned)(0xffff - 8 * tkt - j);
    }
    unsigned m = kk[0];
#pragma unroll
    for (int j = 1; j < 8; ++j) m = umax(m, kk[j]);
    const unsigned M1 = wave_umax(m);
    m = 0;
#pragma unroll
    for (int j = 0; j < 8; ++j) m = umax(m, kk[j] == M1 ? 0u : kk[j]);
    const unsigned M2 = wave_umax(m);
    if (lane == 0) { cand[64 + wave - 8] = M2; if (wave == 8) cand[68] = 0; }
  }
  __syncthreads();
  STAMP(10);
  if (tkw) {
    const unsigned th = min(min(cand[64], cand[65]), min(cand[66], cand[67]));
#pragma unroll
    for (int j = 0; j < 8; ++j) {
      if (kk[j] >= th) {
        const unsigned pos = atomicAdd(&cand[68], 1u);
        if (pos < 64) cand[pos] = kk[j];
      }
    }
  }
  __syncthreads();
  if (wave == 12) {
    const gu64* f = (const gu64*)(ws + WS_F3 + (b % R2) * REP_STRIDE) + lane;
    for (int it = 0; it < SPIN_CAP; ++it) {
      bool ok = true;
#pragma unroll
      for (int i = 0; i < 4; ++i) ok &= __hip_atomic_load(f + 64 * i, RLX_AGENT) == tag;
      if (__all(ok)) break;
      __builtin_amdgcn_s_sleep(1);
    }
  }
  const unsigned ncand = cand[68];
  if (ncand <= 64) {
    if (wave == 0) {
      const unsigned mine = (lane < (int)ncand) ? cand[lane] : 0u;
      unsigned rank = 0;
      for (unsigned i = 0; i < ncand; ++i) rank += (cand[i] > mine) ? 1u : 0u;
      if (lane < (int)ncand && rank < 8) cand[72 + rank] = mine;
    }
  } else {
    for (int rnd = 0; rnd < 8; ++rnd) {
      __syncthreads();
      if (tkw) {
        unsigned m = kk[0];
#pragma unroll
        for (int j = 1; j < 8; ++j) m = umax(m, kk[j]);
        const unsigned M = wave_umax(m);
        if (lane == 0) cand[64 + wave - 8] = M;
      }
      __syncthreads();
      const unsigned M = umax(umax(cand[64], cand[65]), umax(cand[66], cand[67]));
      if (tkw) {
#pragma unroll
        for (int j = 0; j < 8; ++j) kk[j] = (kk[j] == M) ? 0u : kk[j];
      }
      if (tid == 0) cand[72 + rnd] = M;
    }
  }
  __syncthreads();
  STAMP(11);
  if (wave >= 10) {
    for (int rr = wave - 10; rr < 8; rr += 6) {
      const int t = t0 + rr;
      bool routed = false;
#pragma unroll
      for (int j = 0; j < 8; ++j) routed |= (0xffff - (int)(cand[72 + j] & 0xffffu)) == t;
      if (!routed) {
        const unsigned* stg = (const unsigned*)(lds + rr * 2560);
        *(u32x4*)(out + t * D_ + 8 * lane) = *(const u32x4*)(stg + 4 * lane);
        if (lane < 16) *(u32x4*)(out + t * D_ + 512 + 8 * lane) = *(const u32x4*)(stg + 256 + 4 * lane);
      }
    }
  }
  const int tok = lane & 15, g = lane >> 4, i16 = lane & 15;
  float* red = (float*)(lds + L_RED);
  if (b < 216) {
    const int slab = (b & 7) * 27 + (b >> 3);
    const int f0 = slab * 8;
    if (wave < 10) {
      const unsigned mykey = cand[72 + (tok & 7)];
      const int myidx = 0xffff - (int)(mykey & 0xffffu);
      const u32x2 stv = __builtin_amdgcn_raw_buffer_load_b64(rs, WS_STATS + myidx * 8, 0, AUX_SC1);
      u32x4 hraw[2];
#pragma unroll
      for (int i = 0; i < 2; ++i)
        hraw[i] = __builtin_amdgcn_raw_buffer_load_b128(rs, WS_H + (myidx * D_ + 32 * (2 * wave + i) + 8 * g) * 2, 0, AUX_SC1);
      const float sm = __uint_as_float(stv[0]), ss = __uint_as_float(stv[1]);
      const float rcs = __builtin_amdgcn_rcpf(ss);
      f32x4 acc = {0.f, 0.f, 0.f, 0.f};
#pragma unroll
      for (int i = 0; i < 2; ++i) {
        const int k0 = 32 * (2 * wave + i);
        u32x4 HN;
#pragma unroll
        for (int d = 0; d < 4; ++d) {
          const float a = fdiv(rb(lo2f(hraw[i][d]) - sm), ss, rcs);
          const float bb = fdiv(rb(hi2f(hraw[i][d]) - sm), ss, rcs);
          HN[d] = pack2(a, bb);
        }
        const char* p = lds + L_W4 + (k0 + 8 * g + (i16 >> 2)) * 32 + 8 * (i16 & 3);
        const s16x4 blo = tr_read(p);
        const s16x4 bhi = tr_read(p + 4 * 32);
        const bf16x8 bfrag = __builtin_shufflevector(blo, bhi, 0, 1, 2, 3, 4, 5, 6, 7);
        acc = __builtin_amdgcn_mfma_f32_16x16x32_bf16(__builtin_bit_cast(bf16x8, HN), bfrag, acc, 0, 0, 0);
      }
      if (g < 2) {
#pragma unroll
        for (int j = 0; j < 4; ++j) red[(wave * 8 + 4 * g + j) * 16 + i16] = acc[j];
      }
    }
    __syncthreads();
    STAMP(12);
    if (wave == 0) {
      const int tk = lane >> 3, col = lane & 7;
      float G = 0.f, U = 0.f;
#pragma unroll
      for (int w = 0; w < 10; ++w) {
        G += red[(w * 8 + tk) * 16 + col];
        U += red[(w * 8 + tk) * 16 + col + 8];
      }
      G = rb(G); U = rb(U);
      const float gg = rb(G / (1.0f + __expf(-G)));
      u16* stg = (u16*)(lds + L_CAND);
      unsigned gub = f2bf(gg * U);
      if (gub == 0xffffu) gub = 0xfffeu;
      stg[lane] = (u16)gub;
      __builtin_amdgcn_fence(__ATOMIC_RELEASE, "wavefront");
      __builtin_amdgcn_wave_barrier();
      __builtin_amdgcn_fence(__ATOMIC_ACQUIRE, "wavefront");
      {
        const u32x4 o = *(const u32x4*)(stg + 8 * (lane & 7));
        __builtin_amdgcn_raw_buffer_store_b128(o, rs, WS_GU + (lane >> 3) * GU_STRIDE + slab * 128 + (lane & 7) * 16, 0, AUX_SC1);
      }
      if (lane < R4) __hip_atomic_store((gu64*)(ws + WS_F4 + lane * REP_STRIDE) + slab, tag, RLX_AGENT);
      STAMP(13);
    }
  } else {
    const int j = b - 216;
    const int c0 = 16 * ((j & 7) * 5 + (j >> 3));
    unsigned key = 0, hv = 0;
    u32x2 stv = {0u, 0u};
    if (wave < 2) {
      const int tk = tid >> 4;
      key = cand[72 + tk];
      const int idx = 0xffff - (int)(key & 0xffffu);
      hv = (unsigned)(u16)__builtin_amdgcn_raw_buffer_load_b16(rs, WS_H + (idx * D_ + c0 + (tid & 15)) * 2, 0, AUX_SC1);
      stv = __builtin_amdgcn_raw_buffer_load_b64(rs, WS_STATS + idx * 8, 0, AUX_SC1);
    }
    if (wave == 2) {
      const gu64* f = (const gu64*)(ws + WS_F4 + (j % R4) * REP_STRIDE);
      for (int it = 0; it < SPIN_CAP; ++it) {
        bool ok = true;
#pragma unroll
        for (int i = 0; i < 4; ++i) {
          const int fi = lane + 64 * i;
          ok &= __hip_atomic_load(f + (fi < 216 ? fi : 0), RLX_AGENT) == tag;
        }
        if (__all(ok)) break;
        __builtin_amdgcn_s_sleep(1);
      }
    }
    __syncthreads();
    {
      u32x4 af[4];
      for (int it = 0; it < SPIN_CAP; ++it) {
        bool ok = true;
#pragma unroll
        for (int i = 0; i < 4; ++i) {
          const int ks = wave + 16 * i;
          const int ksc = ks < 54 ? ks : 0;
          af[i] = (u32x4){0u, 0u, 0u, 0u};
          if (tok < 8) af[i] = __builtin_amdgcn_raw_buffer_load_b128(rs, WS_GU + (j % R4) * GU_STRIDE + (4 * ksc + g) * 128 + tok * 16, 0, AUX_SC1);
        }
#pragma unroll
        for (int i = 0; i < 4; ++i) ok &= ready4(af[i]);
        if (__all(ok)) break;
        __builtin_amdgcn_s_sleep(2);
      }
      STAMP(12);
      f32x4 acc = {0.f, 0.f, 0.f, 0.f};
#pragma unroll
      for (int i = 0; i < 4; ++i) {
        const int ks = wave + 16 * i;
        if (ks < 54) {
          const int k0 = 32 * ks;
          const char* p = lds + L_W5 + (k0 + 8 * g + (i16 >> 2)) * 32 + 8 * (i16 & 3);
          const s16x4 blo = tr_read(p);
          const s16x4 bhi = tr_read(p + 4 * 32);
          const bf16x8 bfrag = __builtin_shufflevector(blo, bhi, 0, 1, 2, 3, 4, 5, 6, 7);
          acc = __builtin_amdgcn_mfma_f32_16x16x32_bf16(__builtin_bit_cast(bf16x8, af[i]), bfrag, acc, 0, 0, 0);
        }
      }
      if (g < 2) {
#pragma unroll
        for (int jj = 0; jj < 4; ++jj) red[(wave * 8 + 4 * g + jj) * 16 + i16] = acc[jj];
      }
    }
    __syncthreads();
    if (wave < 2) {
      const int tk = tid >> 4, col = tid & 15;
      float Y = 0.f;
#pragma unroll
      for (int w = 0; w < 16; ++w) Y += red[(w * 8 + tk) * 16 + col];
      Y = rb(Y);
      const unsigned ord = key >> 16;
      const unsigned rbits = (ord & 0x8000u) ? (ord & 0x7fffu) : (~ord & 0xffffu);
      const float rv = bf2f(rbits);
      const float rsg = rb(1.0f / (1.0f + __expf(-rv)));
      const float y = rb(Y * rsg);
      const float hval = bf2f(hv);
      const float hn = rb(rb(hval - __uint_as_float(stv[0])) / __uint_as_float(stv[1]));
      const float bo = rb(hn + y);
      const int idx = 0xffff - (int)(key & 0xffffu);
      __builtin_amdgcn_raw_buffer_store_b16((short)f2bf(hval + bo), ro, (idx * D_ + c0 + col) * 2, 0, AUX_SC1);
    }
    STAMP(13);
    if (b == 216 && tid == 0) __hip_atomic_store((gu64*)(ws + WS_E), tag, RLX_AGENT);
  }
}

extern "C" void kernel_launch(void* const* d_in, const int* in_sizes, int n_in, void* d_out, int out_size,
                              void* d_ws, size_t ws_size, hipStream_t stream) {
  fused_kernel<<<256, 1024, 0, stream>>>(
      (const u16*)d_in[0], (const u16*)d_in[1], (const u16*)d_in[2], (const u16*)d_in[3], (const u16*)d_in[4],
      (const u16*)d_in[5], (const u16*)d_in[7], (const u16*)d_in[8], (const u16*)d_in[9], (const u16*)d_in[10],
      (u16*)d_out, (char*)d_ws);
}
```

```cpp
#include <hip/hip_runtime.h>
#include <hip/hip_bf16.h>

typedef unsigned short u16;
typedef unsigned long long u64;
using bf16x8 = __attribute__((ext_vector_type(8))) short;
using s16x4 = __attribute__((ext_vector_type(4))) short;
using f32x16 = __attribute__((ext_vector_type(16))) float;
using f32x4 = __attribute__((ext_vector_type(4))) float;
using u32x4 = __attribute__((ext_vector_type(4))) unsigned;
using u32x2 = __attribute__((ext_vector_type(2))) unsigned;
typedef __attribute__((address_space(1))) u64 gu64;

#define DI __device__ __forceinline__
#define RLX_AGENT __ATOMIC_RELAXED, __HIP_MEMORY_SCOPE_AGENT
#define AUX_SC1 16

static constexpr int S_ = 2048;
static constexpr int D_ = 640;
static constexpr int F_ = 1728;

static constexpr unsigned MAT = S_ * D_ * 2;
static constexpr unsigned WS_KVT = 0;
static constexpr unsigned WS_Y2T = MAT;
static constexpr unsigned WS_H = 2 * MAT;
static constexpr unsigned WS_CTL = 3 * MAT;
static constexpr unsigned WS_E = WS_CTL;
static constexpr unsigned WS_STATS = WS_CTL + 4096;
static constexpr unsigned REP_STRIDE = 4352;
static constexpr int R1 = 4;
static constexpr int R2 = 16;
static constexpr int RK = 16;
static constexpr int R4 = 8;
static constexpr unsigned GU_STRIDE = 28160;
static constexpr unsigned WS_F1 = WS_CTL + 32768;
static constexpr unsigned WS_F2 = WS_F1 + R1 * REP_STRIDE;
static constexpr unsigned WS_F3 = WS_F2 + R2 * REP_STRIDE;
static constexpr unsigned WS_F4 = WS_F3 + R2 * REP_STRIDE;
static constexpr unsigned WS_KEYS = WS_F4 + R4 * REP_STRIDE;
static constexpr unsigned WS_GU = WS_KEYS + RK * REP_STRIDE;
static constexpr unsigned WS_BYTES = WS_GU + R4 * GU_STRIDE;

static constexpr int KC_STRIDE = 4160;
static constexpr int KC_BYTES = 8 * KC_STRIDE;
static constexpr int U_PLANE = 2560;
static constexpr int TEAM = 40960;
static constexpr int L_W4 = 122880;
static constexpr int L_W5 = 81920;
static constexpr int L_TB = 143360;
static constexpr int L_CAND = 154880;
static constexpr int L_RED = 155392;
static constexpr int LDS_BYTES = 163840;

DI float bf2f(unsigned x) { return __uint_as_float(x << 16); }
DI float lo2f(unsigned x) { return __uint_as_float(x << 16); }
DI float hi2f(unsigned x) { return __uint_as_float(x & 0xffff0000u); }
DI unsigned f2bf(float x) {
  __bf16 b = (__bf16)x;
  return (unsigned)__builtin_bit_cast(unsigned short, b);
}
DI float rb(float x) { return (float)((__bf16)x); }
DI unsigned pack2(float lo, float hi) { return f2bf(lo) | (f2bf(hi) << 16); }
typedef float f32x2 __attribute__((ext_vector_type(2)));
typedef __bf16 bf16x2_t __attribute__((ext_vector_type(2)));
DI f32x2 unpk(unsigned x) { return (f32x2){lo2f(x), hi2f(x)}; }
DI unsigned pk(f32x2 v) { return __builtin_bit_cast(unsigned, __builtin_convertvector(v, bf16x2_t)); }
DI f32x2 rb2(f32x2 v) { return unpk(pk(v)); }
DI unsigned nosent(unsigned p) {
  if ((p & 0xffffu) == 0xffffu) p ^= 1u;
  if ((p >> 16) == 0xffffu) p ^= 0x10000u;
  return p;
}
DI bool ready4(const u32x4 a) {
  bool ok = true;
#pragma unroll
  for (int d = 0; d < 4; ++d) ok &= ((a[d] & 0xffffu) != 0xffffu) && ((a[d] >> 16) != 0xffffu);
  return ok;
}
DI float fdiv(float a, float b, float rcpb) {
  const float q0 = a * rcpb;
  const float e = __builtin_fmaf(-q0, b, a);
  return __builtin_fmaf(e, rcpb, q0);
}

template <int CTRL>
DI int dpp_i(int v) { return __builtin_amdgcn_update_dpp(0, v, CTRL, 0xF, 0xF, true); }
DI float wave_sum(float v) {
  v += __int_as_float(dpp_i<0xB1>(__float_as_int(v)));
  v += __int_as_float(dpp_i<0x4E>(__float_as_int(v)));
  v += __int_as_float(dpp_i<0x141>(__float_as_int(v)));
  v += __int_as_float(dpp_i<0x140>(__float_as_int(v)));
  float a = __int_as_float(__builtin_amdgcn_readlane(__float_as_int(v), 0));
  float b = __int_as_float(__builtin_amdgcn_readlane(__float_as_int(v), 16));
  float c = __int_as_float(__builtin_amdgcn_readlane(__float_as_int(v), 32));
  float d = __int_as_float(__builtin_amdgcn_readlane(__float_as_int(v), 48));
  return (a + b) + (c + d);
}
DI unsigned umax(unsigned a, unsigned b) { return a > b ? a : b; }
DI unsigned wave_umax(unsigned v) {
  v = umax(v, (unsigned)dpp_i<0xB1>((int)v));
  v = umax(v, (unsigned)dpp_i<0x4E>((int)v));
  v = umax(v, (unsigned)dpp_i<0x141>((int)v));
  v = umax(v, (unsigned)dpp_i<0x140>((int)v));
  unsigned a = (unsigned)__builtin_amdgcn_readlane((int)v, 0);
  unsigned b = (unsigned)__builtin_amdgcn_readlane((int)v, 16);
  unsigned c = (unsigned)__builtin_amdgcn_readlane((int)v, 32);
  unsigned d = (unsigned)__builtin_amdgcn_readlane((int)v, 48);
  return umax(umax(a, b), umax(c, d));
}
typedef __attribute__((ext_vector_type(4))) short v4i16_t;
DI s16x4 tr_read(const char* p) {
  return __builtin_bit_cast(s16x4, __builtin_amdgcn_ds_read_tr16_b64_v4i16(
      (__attribute__((address_space(3))) v4i16_t*)p));
}
DI void glds16(const void* gsrc, unsigned lds_dst) {
  unsigned keep;
  asm volatile("s_mov_b32 %0, m0\n\ts_mov_b32 m0, %2\n\ts_nop 0\n\tglobal_load_lds_dwordx4 %1, off\n\ts_mov_b32 m0, %0"
               : "=&s"(keep) : "v"(gsrc), "s"(lds_dst) : "memory");
}
DI void drain() { asm volatile("s_waitcnt vmcnt(0)" ::: "memory"); }
static constexpr int SPIN_CAP = 1 << 20;
#define STAMP(i) do {} while (0)

template <int MASK>
DI void conv_tiles(const char* kc, const char* us, char* red, int w, int lane) {
  const int rp = lane & 15, g = lane >> 4;
  const char* pa = kc + (rp & 7) * KC_STRIDE + 2 * (2040 - 8 * (rp >> 3) + 8 * g) - 64 * w;
  const char* pb = us + (g >> 1) * U_PLANE + (rp + 16 - w) * 32 + 16 * (g & 1);
  f32x4 acc[4][2];
#pragma unroll
  for (int a = 0; a < 4; ++a) { acc[a][0] = (f32x4){0.f, 0.f, 0.f, 0.f}; acc[a][1] = (f32x4){0.f, 0.f, 0.f, 0.f}; }
#pragma unroll
  for (int mi = 0; mi < 16; ++mi) {
    constexpr int dummy = 0; (void)dummy;
    const int amin = mi >> 2;
    if ((MASK >> amin) == 0) continue;
    const bf16x8 a0 = *(const bf16x8*)(pa - 256 * mi);
    const bf16x8 a1 = *(const bf16x8*)(pa - 256 * mi - 32);
#pragma unroll
    for (int a = 0; a < 4; ++a) {
      if (a >= amin && ((MASK >> a) & 1)) {
        const bf16x8 bb = *(const bf16x8*)(pb + 512 * a - 128 * mi);
        acc[a][0] = __builtin_amdgcn_mfma_f32_16x16x32_bf16(a0, bb, acc[a][0], 0, 0, 0);
        acc[a][1] = __builtin_amdgcn_mfma_f32_16x16x32_bf16(a1, bb, acc[a][1], 0, 0, 0);
      }
    }
  }
  __syncthreads();
  char* rd = red + w * 9216 + rp * 144 + 16 * g;
#pragma unroll
  for (int a = 0; a < 4; ++a) {
    if ((MASK >> a) & 1) {
      *(f32x4*)(rd + a * 16 * 144) = acc[a][0];
      *(f32x4*)(rd + a * 16 * 144 + 64) = acc[a][1];
    }
  }
}

__global__ __launch_bounds__(1024) void fused_kernel(
    const u16* __restrict__ q, const u16* __restrict__ k, const u16* __restrict__ v,
    const u16* __restrict__ shift_k, const u16* __restrict__ ssm_k, const u16* __restrict__ ssm_D,
    const u16* __restrict__ w_r, const u16* __restrict__ w_gate, const u16* __restrict__ w_up,
    const u16* __restrict__ w_down, u16* out, char* ws) {
  __shared__ __attribute__((aligned(16))) char lds[LDS_BYTES];
  asm volatile("" ::"s"(q), "s"(k), "s"(v), "s"(shift_k), "s"(ssm_k), "s"(ssm_D), "s"(w_r), "s"(w_gate), "s"(w_up),
               "s"(w_down), "s"(out), "s"(ws));
  const int b = blockIdx.x;
  const int grp = b & 7, ib = b >> 3;
  const int tid = threadIdx.x, lane = tid & 63;
  const int wave = __builtin_amdgcn_readfirstlane(tid >> 6);
  const int team = wave >> 2, tt = tid & 255;
  const __amdgpu_buffer_rsrc_t rs = __builtin_amdgcn_make_buffer_rsrc(ws, 0, WS_BYTES, 0x00020000);
  const __amdgpu_buffer_rsrc_t ro = __builtin_amdgcn_make_buffer_rsrc(out, 0, MAT, 0x00020000);
  const u32x4 ones = {0xffffffffu, 0xffffffffu, 0xffffffffu, 0xffffffffu};
  STAMP(0);

  const int p1tok = tid / 10, p1chunk = tid - 10 * p1tok;
  const int p1c = 80 * grp + 8 * p1chunk;
  const bool conv_on = team < 3;
  const int chbase = 80 * grp + 5 * (ib >> 1);
  const int odd = ib & 1;
  const int ch = chbase + (team >= 2 ? 2 : (odd ? 3 + team : team));
  const int t0 = (grp * 32 + ib) * 8;
  const unsigned lbase = (unsigned)(size_t)lds;
  u32x4 kown = {0u, 0u, 0u, 0u}, vr = {0u, 0u, 0u, 0u};
  u64 eraw = 0;
  if (wave < 10) {
    const int t = 64 * ib + p1tok;
    kown = *(const u32x4*)(k + (size_t)t * D_ + p1c);
    vr = *(const u32x4*)(v + (size_t)t * D_ + p1c);
    if (wave == 0) eraw = __hip_atomic_load((gu64*)(ws + WS_E), RLX_AGENT);
    *(u32x4*)(lds + (p1tok + 3) * 176 + 16 * p1chunk) = kown;
    asm volatile("" : "+v"(vr), "+v"(eraw));
  } else if (wave == 10) {
    if (lane < 30) {
      const int hr = lane / 10, hc = lane - 10 * hr;
      const int t = 64 * ib - 3 + hr;
      u32x4 kh = {0u, 0u, 0u, 0u};
      if (t >= 0) kh = *(const u32x4*)(k + (size_t)t * D_ + 80 * grp + 8 * hc);
      *(u32x4*)(lds + hr * 176 + 16 * hc) = kh;
    }
    if (b < 216)
      __builtin_amdgcn_raw_buffer_store_b128(ones, rs, WS_GU + (lane >> 3) * GU_STRIDE + ((b & 7) * 27 + (b >> 3)) * 128 + (lane & 7) * 16, 0, AUX_SC1);
  } else if (wave == 11) {
    if (lane < 40) {
      const u32x4 d = *(const u32x4*)(shift_k + (size_t)grp * 320 + 8 * lane);
      *(f32x4*)(lds + 12288 + 32 * lane) = (f32x4){lo2f(d[0]), lo2f(d[2]), hi2f(d[0]), hi2f(d[2])};
      *(f32x4*)(lds + 12288 + 32 * lane + 16) = (f32x4){lo2f(d[1]), lo2f(d[3]), hi2f(d[1]), hi2f(d[3])};
    }
    if (lane < RK) __builtin_amdgcn_raw_buffer_store_b128(ones, rs, WS_KEYS + lane * REP_STRIDE + t0 * 2, 0, AUX_SC1);
  } else if (wave == 15) {
    const char* tp = ws + (size_t)(lane & 31) * (WS_BYTES / 32 & ~15u);
    if (lane >= 32) tp = (const char*)out + (size_t)(lane & 31) * (MAT / 32);
    glds16(tp, lbase + L_RED);
  } else {
    const int chk = chbase + (wave == 14 ? 2 : (odd ? 3 + (wave - 12) : (wave - 12)));
#pragma unroll
    for (int i = 0; i < 4; ++i)
      glds16(ssm_k + (size_t)chk * S_ + 512 * i + 8 * lane, lbase + L_W4 + 4096 * (wave - 12) + 1024 * i);
  }
  __syncthreads();
  unsigned Draw = 0;
  if (wave < 10) {
    u32x4 kr[3];
#pragma unroll
    for (int i = 0; i < 3; ++i) kr[i] = *(const u32x4*)(lds + (p1tok + i) * 176 + 16 * p1chunk);
#pragma unroll
    for (int jp = 0; jp < 4; ++jp) {
      const f32x4 wa = *(const f32x4*)(lds + 12288 + 128 * p1chunk + 32 * jp);
      const f32x4 wb = *(const f32x4*)(lds + 12288 + 128 * p1chunk + 32 * jp + 16);
      const f32x2 w0 = {wa[0], wa[1]}, w1 = {wa[2], wa[3]}, w2 = {wb[0], wb[1]}, w3 = {wb[2], wb[3]};
      f32x2 ks = rb2(unpk(kr[0][jp]) * w3);
      ks = rb2(ks + rb2(unpk(kr[1][jp]) * w2));
      ks = rb2(ks + rb2(unpk(kr[2][jp]) * w1));
      ks = rb2(ks + rb2(unpk(kown[jp]) * w0));
      const unsigned o = pk(ks * unpk(vr[jp]));
      char* dst = lds + L_TB + (8 * p1chunk + 2 * jp) * 128 + 16 * p1chunk + 2 * p1tok;
      *(u16*)dst = (u16)(o & 0xffffu);
      *(u16*)(dst + 128) = (u16)(o >> 16);
    }
  }
  asm volatile("" : "+v"(eraw));
  const u64 tag0 = eraw + 1ull;
  if (tid == 0) { *(u64*)(lds + L_CAND + 384) = tag0; *(unsigned*)(lds + L_CAND + 400) = 0u; }
  if (wave >= 12) drain();
  __syncthreads();
  STAMP(1);
  if (tid < 640) {
    const int row = tid >> 3, piece = tid & 7;
    const u32x4 d = *(const u32x4*)(lds + L_TB + row * 128 + 16 * (row >> 3) + 16 * piece);
    __builtin_amdgcn_raw_buffer_store_b128(d, rs, WS_KVT + ((80 * grp + row) * S_ + 64 * ib + 8 * piece) * 2, 0, AUX_SC1);
  }
  const u64 tag = *(const u64*)(lds + L_CAND + 384);
  char* kc = lds + TEAM * team;
  char* us = kc + KC_BYTES;
  if (conv_on) {
    const u32x4 z4 = {0u, 0u, 0u, 0u};
    u32x4 klo = *(const u32x4*)(lds + L_W4 + 4096 * team + 16 * (tt < 255 ? 254 - tt : 0));
    const u32x4 khi = *(const u32x4*)(lds + L_W4 + 4096 * team + 16 * (255 - tt));
    if (tt >= 255) klo = z4;
    if (tt < 64) *(u32x4*)(us + (tt >> 5) * U_PLANE + 16 * (tt & 31)) = z4;
    if (tt < 32) *(u32x4*)(kc + (tt >> 2) * KC_STRIDE + 16 * (256 + (tt & 3))) = z4;
    const unsigned W[8] = {klo[0], klo[1], klo[2], klo[3], khi[0], khi[1], khi[2], khi[3]};
    unsigned E[7], O[7];
#pragma unroll
    for (int d = 0; d < 7; ++d) {
      E[d] = (W[7 - d] >> 16) | (W[7 - d] << 16);
      O[d] = (W[7 - d] & 0xffffu) | (W[6 - d] & 0xffff0000u);
    }
    char* dst = kc + 16 * tt;
#pragma unroll
    for (int qq = 0; qq < 4; ++qq) {
      *(u32x4*)(dst + (7 - 2 * qq) * KC_STRIDE) = (u32x4){E[qq], E[qq + 1], E[qq + 2], E[qq + 3]};
      *(u32x4*)(dst + (6 - 2 * qq) * KC_STRIDE) = (u32x4){O[qq], O[qq + 1], O[qq + 2], O[qq + 3]};
    }
  }
  if (wave < 12) drain();
  __syncthreads();
  if (tid < R1) __hip_atomic_store((gu64*)(ws + WS_F1 + tid * REP_STRIDE) + grp * 32 + ib, tag, RLX_AGENT);
  STAMP(2);

  unsigned xr[5], wr[5];
  if (wave < 8) {
    const int t = t0 + wave;
#pragma unroll
    for (int i = 0; i < 5; ++i) xr[i] = *(const unsigned*)(q + (size_t)t * D_ + 2 * lane + 128 * i);
#pragma unroll
    for (int i = 0; i < 5; ++i) wr[i] = *(const unsigned*)(w_r + 2 * lane + 128 * i);
  }

  if (wave == 12) {
    const gu64* f = (const gu64*)(ws + WS_F1 + (ib % R1) * REP_STRIDE) + grp * 32 + (lane & 31);
    for (int it = 0; it < SPIN_CAP; ++it) {
      const bool ok = __hip_atomic_load(f, RLX_AGENT) == tag;
      if (__all(ok)) break;
      __builtin_amdgcn_s_sleep(1);
    }
  }
  __syncthreads();
  STAMP(3);
  u32x4 uu = {0u, 0u, 0u, 0u};
  if (conv_on) {
    Draw = ssm_D[ch];
    uu = __builtin_amdgcn_raw_buffer_load_b128(rs, WS_KVT + (ch * S_ + 8 * tt) * 2, 0, AUX_SC1);
    *(u32x4*)(us + ((tt >> 1) & 1) * U_PLANE + ((tt >> 2) + 16) * 32 + 16 * (tt & 1)) = uu;
  }
  __syncthreads();
  STAMP(4);
  if (b < 216) {
    if (wave >= 12) {
      const int f0 = ((b & 7) * 27 + (b >> 3)) * 8;
#pragma unroll
      for (int i = 0; i < 5; ++i) {
        const int p = tt + 256 * i;
        const u16* src = ((p & 1) ? w_up : w_gate) + (size_t)(p >> 1) * F_ + f0;
        glds16(src, lbase + L_W4 + 16 * ((wave - 12) * 64 + 256 * i));
      }
    }
  }
  if (team < 2) conv_tiles<15>(kc, us, kc, wave & 3, lane);
  else if (team == 2) { if (odd) conv_tiles<6>(kc, us, kc, wave & 3, lane); else conv_tiles<9>(kc, us, kc, wave & 3, lane); }
  else __syncthreads();
  STAMP(5);
  __syncthreads();
  const int myal = tt >> 6;
  if (team < 2 || (team == 2 && ((myal == 1 || myal == 2) == (odd != 0)))) {
    const char* red = kc + (tt >> 2) * 144 + (tt & 3) * 32;
    f32x4 s0 = *(const f32x4*)(red), s1 = *(const f32x4*)(red + 16);
#pragma unroll
    for (int w = 1; w < 4; ++w) {
      s0 += *(const f32x4*)(red + w * 9216);
      s1 += *(const f32x4*)(red + w * 9216 + 16);
    }
    const float Dc = bf2f(Draw);
    float o[8];
#pragma unroll
    for (int j = 0; j < 8; ++j) {
      const float acc = j < 4 ? s0[j] : s1[j - 4];
      const float kv = (j & 1) ? hi2f(uu[j >> 1]) : lo2f(uu[j >> 1]);
      o[j] = rb(acc) + rb(kv * Dc);
    }
    const u32x4 st = {pack2(o[0], o[1]), pack2(o[2], o[3]), pack2(o[4], o[5]), pack2(o[6], o[7])};
    __builtin_amdgcn_raw_buffer_store_b128(st, rs, WS_Y2T + (ch * S_ + 8 * tt) * 2, 0, AUX_SC1);
  }
  if (conv_on) drain();
  __syncthreads();
  if (tid < R2) __hip_atomic_store((gu64*)(ws + WS_F2 + tid * REP_STRIDE) + b, tag, RLX_AGENT);
  STAMP(6);
  if (b >= 216 && wave >= 13) {
    const int j5 = b - 216;
    const int c05 = 16 * ((j5 & 7) * 5 + (j5 >> 3));
#pragma unroll
    for (int i = 0; i < 6; ++i) {
      const int pb = ((0 * 6 + i) * 3 + (wave - 13)) * 64;
      const int p = pb + lane;
      glds16(w_down + (size_t)(p >> 1) * D_ + c05 + 8 * (p & 1), lbase + L_W5 + 16 * pb);
    }
  }

  float xf[10], ql[10];
  if (wave < 8) {
#pragma unroll
    for (int i = 0; i < 5; ++i) { xf[2 * i] = lo2f(xr[i]); xf[2 * i + 1] = hi2f(xr[i]); }
    float s = 0.f;
#pragma unroll
    for (int i = 0; i < 10; ++i) s += xf[i];
    const float mean1 = wave_sum(s) * (1.0f / D_);
    float qq = 0.f;
#pragma unroll
    for (int i = 0; i < 10; ++i) { const float d = xf[i] - mean1; qq += d * d; }
    const float var1 = wave_sum(qq) * (1.0f / D_);
    const float m1 = rb(mean1);
    const float sd1 = rb(sqrtf(rb(rb(var1) + 1e-5f)));
    const float rc1 = __builtin_amdgcn_rcpf(sd1);
#pragma unroll
    for (int i = 0; i < 10; ++i) ql[i] = rb(fdiv(rb(xf[i] - m1), sd1, rc1));
    asm volatile("" : "+v"(ql[0]), "+v"(ql[1]), "+v"(ql[2]), "+v"(ql[3]), "+v"(ql[4]), "+v"(ql[5]), "+v"(ql[6]),
                 "+v"(ql[7]), "+v"(ql[8]), "+v"(ql[9]));
  }

  if (wave == 11) {
    const gu64* f = (const gu64*)(ws + WS_F2 + (b % R2) * REP_STRIDE) + lane;
    for (int it = 0; it < SPIN_CAP; ++it) {
      bool ok = true;
#pragma unroll
      for (int i = 0; i < 4; ++i) ok &= __hip_atomic_load(f + 64 * i, RLX_AGENT) == tag;
      if (__all(ok)) break;
      __builtin_amdgcn_s_sleep(1);
    }
  }
  __syncthreads();
  STAMP(7);
  {
    u16* ys = (u16*)(lds + L_TB);
    if (wave < 10) {
      const u32x4 a = __builtin_amdgcn_raw_buffer_load_b128(rs, WS_Y2T + (tid * S_ + t0) * 2, 0, AUX_SC1);
#pragma unroll
      for (int j = 0; j < 8; ++j) ys[j * 648 + tid] = (u16)((j & 1) ? (a[j >> 1] >> 16) : (a[j >> 1] & 0xffffu));
    }
    __syncthreads();
    STAMP(8);
  if (b >= 216 && wave >= 13) {
    const int j5 = b - 216;
    const int c05 = 16 * ((j5 & 7) * 5 + (j5 >> 3));
#pragma unroll
    for (int i = 0; i < 6; ++i) {
      const int pb = ((1 * 6 + i) * 3 + (wave - 13)) * 64;
      const int p = pb + lane;
      glds16(w_down + (size_t)(p >> 1) * D_ + c05 + 8 * (p & 1), lbase + L_W5 + 16 * pb);
    }
  }
    float hf[10], hn[10];
    float m2 = 0.f, sd2 = 1.f;
    if (wave < 8) {
#pragma unroll
      for (int i = 0; i < 5; ++i) {
        const unsigned yy = *(const unsigned*)&ys[wave * 648 + 2 * lane + 128 * i];
        hf[2 * i] = rb(xf[2 * i] + rb(ql[2 * i] * lo2f(yy)));
        hf[2 * i + 1] = rb(xf[2 * i + 1] + rb(ql[2 * i + 1] * hi2f(yy)));
      }
      float s = 0.f;
#pragma unroll
      for (int i = 0; i < 10; ++i) s += hf[i];
      const float mean2 = wave_sum(s) * (1.0f / D_);
      float qq = 0.f;
#pragma unroll
      for (int i = 0; i < 10; ++i) { const float d = hf[i] - mean2; qq += d * d; }
      const float var2 = wave_sum(qq) * (1.0f / D_);
      m2 = rb(mean2);
      sd2 = rb(sqrtf(rb(rb(var2) + 1e-5f)));
      const float rc2 = __builtin_amdgcn_rcpf(sd2);
      float dot = 0.f;
#pragma unroll
      for (int i = 0; i < 5; ++i) {
        hn[2 * i] = rb(fdiv(rb(hf[2 * i] - m2), sd2, rc2));
        hn[2 * i + 1] = rb(fdiv(rb(hf[2 * i + 1] - m2), sd2, rc2));
        dot += hn[2 * i] * lo2f(wr[i]) + hn[2 * i + 1] * hi2f(wr[i]);
      }
      const float rr = wave_sum(dot);
      unsigned rbits = f2bf(rr);
      if (rbits == 0xffffu) rbits = 0xfffeu;
      if (lane == 0) ((u16*)(lds + L_CAND))[wave] = (u16)rbits;
    }
    __syncthreads();
  if (b >= 216 && wave >= 13) {
    const int j5 = b - 216;
    const int c05 = 16 * ((j5 & 7) * 5 + (j5 >> 3));
#pragma unroll
    for (int i = 0; i < 6; ++i) {
      const int pb = ((2 * 6 + i) * 3 + (wave - 13)) * 64;
      const int p = pb + lane;
      glds16(w_down + (size_t)(p >> 1) * D_ + c05 + 8 * (p & 1), lbase + L_W5 + 16 * pb);
    }
  }
    if (wave == 8 && lane < RK) {
      const u32x4 kq = *(const u32x4*)(lds + L_CAND);
      __builtin_amdgcn_raw_buffer_store_b128(kq, rs, WS_KEYS + lane * REP_STRIDE + t0 * 2, 0, AUX_SC1);
    }
    if (wave < 8) {
      const int t = t0 + wave;
      unsigned* stg = (unsigned*)(lds + wave * 2560);
#pragma unroll
      for (int i = 0; i < 5; ++i) {
        stg[lane + 64 * i] = pack2(hf[2 * i] + hn[2 * i], hf[2 * i + 1] + hn[2 * i + 1]);
        stg[320 + lane + 64 * i] = pack2(hf[2 * i], hf[2 * i + 1]);
      }
      __builtin_amdgcn_fence(__ATOMIC_RELEASE, "wavefront");
      __builtin_amdgcn_wave_barrier();
      __builtin_amdgcn_fence(__ATOMIC_ACQUIRE, "wavefront");
      const u32x4 h0 = *(const u32x4*)(stg + 320 + 4 * lane);
      __builtin_amdgcn_raw_buffer_store_b128(h0, rs, WS_H + (t * D_ + 8 * lane) * 2, 0, AUX_SC1);
      if (lane < 16) {
        const u32x4 h1 = *(const u32x4*)(stg + 320 + 256 + 4 * lane);
        __builtin_amdgcn_raw_buffer_store_b128(h1, rs, WS_H + (t * D_ + 512 + 8 * lane) * 2, 0, AUX_SC1);
      }
      if (lane == 0) {
        const u32x2 sv = {__float_as_uint(m2), __float_as_uint(sd2)};
        __builtin_amdgcn_raw_buffer_store_b64(sv, rs, WS_STATS + t * 8, 0, AUX_SC1);
      }
    }
    drain();
    if (wave < 8) {
      unsigned old = 0;
      if (lane == 0) old = atomicAdd((unsigned*)(lds + L_CAND + 400), 1u);
      old = __builtin_amdgcn_readfirstlane(old);
      if (old == 7u && lane < R2) __hip_atomic_store((gu64*)(ws + WS_F3 + lane * REP_STRIDE) + b, tag, RLX_AGENT);
    }
  }
  STAMP(9);

  unsigned* cand = (unsigned*)(lds + L_CAND);
  unsigned kk[8];
  const bool tkw = (wave >= 8 && wave < 12);
  const int tkt = tid - 512;
  if (tkw) {
    u32x4 gq = ones;
    __builtin_amdgcn_s_sleep(24);
    for (int it = 0; it < SPIN_CAP; ++it) {
      gq = __builtin_amdgcn_raw_buffer_load_b128(rs, WS_KEYS + (b % RK) * REP_STRIDE + tkt * 16, 0, AUX_SC1);
      if (__all(ready4(gq))) break;
      __builtin_amdgcn_s_sleep(10);
    }
#pragma unroll
    for (int j = 0; j < 8; ++j) {
      unsigned x = (j & 1) ? (gq[j >> 1] >> 16) : (gq[j >> 1] & 0xffffu);
      x = (x & 0x8000u) ? (~x & 0xffffu) : (x | 0x8000u);
      kk[j] = (x << 16) | (unsigned)(0xffff - 8 * tkt - j);
    }
    unsigned m = kk[0];
#pragma unroll
    for (int j = 1; j < 8; ++j) m = umax(m, kk[j]);
    const unsigned M1 = wave_umax(m);
    m = 0;
#pragma unroll
    for (int j = 0; j < 8; ++j) m = umax(m, kk[j] == M1 ? 0u : kk[j]);
    const unsigned M2 = wave_umax(m);
    if (lane == 0) { cand[64 + wave - 8] = M2; if (wave == 8) cand[68] = 0; }
  }
  __syncthreads();
  STAMP(10);
  if (tkw) {
    const unsigned th = min(min(cand[64], cand[65]), min(cand[66], cand[67]));
#pragma unroll
    for (int j = 0; j < 8; ++j) {
      if (kk[j] >= th) {
        const unsigned pos = atomicAdd(&cand[68], 1u);
        if (pos < 64) cand[pos] = kk[j];
      }
    }
  }
  __syncthreads();
  if (wave == 12) {
    const gu64* f = (const gu64*)(ws + WS_F3 + (b % R2) * REP_STRIDE) + lane;
    for (int it = 0; it < SPIN_CAP; ++it) {
      bool ok = true;
#pragma unroll
      for (int i = 0; i < 4; ++i) ok &= __hip_atomic_load(f + 64 * i, RLX_AGENT) == tag;
      if (__all(ok)) break;
      __builtin_amdgcn_s_sleep(1);
    }
  }
  const unsigned ncand = cand[68];
  if (ncand <= 64) {
    if (wave == 0) {
      const unsigned mine = (lane < (int)ncand) ? cand[lane] : 0u;
      unsigned rank = 0;
      for (unsigned i = 0; i < ncand; ++i) rank += (cand[i] > mine) ? 1u : 0u;
      if (lane < (int)ncand && rank < 8) cand[72 + rank] = mine;
    }
  } else {
    for (int rnd = 0; rnd < 8; ++rnd) {
      __syncthreads();
      if (tkw) {
        unsigned m = kk[0];
#pragma unroll
        for (int j = 1; j < 8; ++j) m = umax(m, kk[j]);
        const unsigned M = wave_umax(m);
        if (lane == 0) cand[64 + wave - 8] = M;
      }
      __syncthreads();
      const unsigned M = umax(umax(cand[64], cand[65]), umax(cand[66], cand[67]));
      if (tkw) {
#pragma unroll
        for (int j = 0; j < 8; ++j) kk[j] = (kk[j] == M) ? 0u : kk[j];
      }
      if (tid == 0) cand[72 + rnd] = M;
    }
  }
  __syncthreads();
  STAMP(11);
  if (wave >= 10) {
    for (int rr = wave - 10; rr < 8; rr += 6) {
      const int t = t0 + rr;
      bool routed = false;
#pragma unroll
      for (int j = 0; j < 8; ++j) routed |= (0xffff - (int)(cand[72 + j] & 0xffffu)) == t;
      if (!routed) {
        const unsigned* stg = (const unsigned*)(lds + rr * 2560);
        *(u32x4*)(out + t * D_ + 8 * lane) = *(const u32x4*)(stg + 4 * lane);
        if (lane < 16) *(u32x4*)(out + t * D_ + 512 + 8 * lane) = *(const u32x4*)(stg + 256 + 4 * lane);
      }
    }
  }
  const int tok = lane & 15, g = lane >> 4, i16 = lane & 15;
  float* red = (float*)(lds + L_RED);
  if (b < 216) {
    const int slab = (b & 7) * 27 + (b >> 3);
    const int f0 = slab * 8;
    if (wave < 10) {
      const unsigned mykey = cand[72 + (tok & 7)];
      const int myidx = 0xffff - (int)(mykey & 0xffffu);
      const u32x2 stv = __builtin_amdgcn_raw_buffer_load_b64(rs, WS_STATS + myidx * 8, 0, AUX_SC1);
      u32x4 hraw[2];
#pragma unroll
      for (int i = 0; i < 2; ++i)
        hraw[i] = __builtin_amdgcn_raw_buffer_load_b128(rs, WS_H + (myidx * D_ + 32 * (2 * wave + i) + 8 * g) * 2, 0, AUX_SC1);
      const float sm = __uint_as_float(stv[0]), ss = __uint_as_float(stv[1]);
      const float rcs = __builtin_amdgcn_rcpf(ss);
      f32x4 acc = {0.f, 0.f, 0.f, 0.f};
#pragma unroll
      for (int i = 0; i < 2; ++i) {
        const int k0 = 32 * (2 * wave + i);
        u32x4 HN;
#pragma unroll
        for (int d = 0; d < 4; ++d) {
          const float a = fdiv(rb(lo2f(hraw[i][d]) - sm), ss, rcs);
          const float bb = fdiv(rb(hi2f(hraw[i][d]) - sm), ss, rcs);
          HN[d] = pack2(a, bb);
        }
        const char* p = lds + L_W4 + (k0 + 8 * g + (i16 >> 2)) * 32 + 8 * (i16 & 3);
        const s16x4 blo = tr_read(p);
        const s16x4 bhi = tr_read(p + 4 * 32);
        const bf16x8 bfrag = __builtin_shufflevector(blo, bhi, 0, 1, 2, 3, 4, 5, 6, 7);
        acc = __builtin_amdgcn_mfma_f32_16x16x32_bf16(__builtin_bit_cast(bf16x8, HN), bfrag, acc, 0, 0, 0);
      }
      if (g < 2) {
#pragma unroll
        for (int j = 0; j < 4; ++j) red[(wave * 8 + 4 * g + j) * 16 + i16] = acc[j];
      }
    }
    __syncthreads();
    STAMP(12);
    if (wave == 0) {
      const int tk = lane >> 3, col = lane & 7;
      float G = 0.f, U = 0.f;
#pragma unroll
      for (int w = 0; w < 10; ++w) {
        G += red[(w * 8 + tk) * 16 + col];
        U += red[(w * 8 + tk) * 16 + col + 8];
      }
      G = rb(G); U = rb(U);
      const float gg = rb(G / (1.0f + __expf(-G)));
      u16* stg = (u16*)(lds + L_CAND);
      unsigned gub = f2bf(gg * U);
      if (gub == 0xffffu) gub = 0xfffeu;
      stg[lane] = (u16)gub;
      __builtin_amdgcn_fence(__ATOMIC_RELEASE, "wavefront");
      __builtin_amdgcn_wave_barrier();
      __builtin_amdgcn_fence(__ATOMIC_ACQUIRE, "wavefront");
      {
        const u32x4 o = *(const u32x4*)(stg + 8 * (lane & 7));
        __builtin_amdgcn_raw_buffer_store_b128(o, rs, WS_GU + (lane >> 3) * GU_STRIDE + slab * 128 + (lane & 7) * 16, 0, AUX_SC1);
      }
      if (lane < R4) __hip_atomic_store((gu64*)(ws + WS_F4 + lane * REP_STRIDE) + slab, tag, RLX_AGENT);
      STAMP(13);
    }
  } else {
    const int j = b - 216;
    const int c0 = 16 * ((j & 7) * 5 + (j >> 3));
    unsigned key = 0, hv = 0;
    u32x2 stv = {0u, 0u};
    if (wave < 2) {
      const int tk = tid >> 4;
      key = cand[72 + tk];
      const int idx = 0xffff - (int)(key & 0xffffu);
      hv = (unsigned)(u16)__builtin_amdgcn_raw_buffer_load_b16(rs, WS_H + (idx * D_ + c0 + (tid & 15)) * 2, 0, AUX_SC1);
      stv = __builtin_amdgcn_raw_buffer_load_b64(rs, WS_STATS + idx * 8, 0, AUX_SC1);
    }
    if (wave == 2) {
      const gu64* f = (const gu64*)(ws + WS_F4 + (j % R4) * REP_STRIDE);
      for (int it = 0; it < SPIN_CAP; ++it) {
        bool ok = true;
#pragma unroll
        for (int i = 0; i < 4; ++i) {
          const int fi = lane + 64 * i;
          ok &= __hip_atomic_load(f + (fi < 216 ? fi : 0), RLX_AGENT) == tag;
        }
        if (__all(ok)) break;
        __builtin_amdgcn_s_sleep(1);
      }
    }
    __syncthreads();
    {
      u32x4 af[4];
      for (int it = 0; it < SPIN_CAP; ++it) {
        bool ok = true;
#pragma unroll
        for (int i = 0; i < 4; ++i) {
          const int ks = wave + 16 * i;
          const int ksc = ks < 54 ? ks : 0;
          af[i] = (u32x4){0u, 0u, 0u, 0u};
          if (tok < 8) af[i] = __builtin_amdgcn_raw_buffer_load_b128(rs, WS_GU + (j % R4) * GU_STRIDE + (4 * ksc + g) * 128 + tok * 16, 0, AUX_SC1);
        }
#pragma unroll
        for (int i = 0; i < 4; ++i) ok &= ready4(af[i]);
        if (__all(ok)) break;
        __builtin_amdgcn_s_sleep(2);
      }
      STAMP(12);
      f32x4 acc = {0.f, 0.f, 0.f, 0.f};
#pragma unroll
      for (int i = 0; i < 4; ++i) {
        const int ks = wave + 16 * i;
        if (ks < 54) {
          const int k0 = 32 * ks;
          const char* p = lds + L_W5 + (k0 + 8 * g + (i16 >> 2)) * 32 + 8 * (i16 & 3);
          const s16x4 blo = tr_read(p);
          const s16x4 bhi = tr_read(p + 4 * 32);
          const bf16x8 bfrag = __builtin_shufflevector(blo, bhi, 0, 1, 2, 3, 4, 5, 6, 7);
          acc = __builtin_amdgcn_mfma_f32_16x16x32_bf16(__builtin_bit_cast(bf16x8, af[i]), bfrag, acc, 0, 0, 0);
        }
      }
      if (g < 2) {
#pragma unroll
        for (int jj = 0; jj < 4; ++jj) red[(wave * 8 + 4 * g + jj) * 16 + i16] = acc[jj];
      }
    }
    __syncthreads();
    if (wave < 2) {
      const int tk = tid >> 4, col = tid & 15;
      float Y = 0.f;
#pragma unroll
      for (int w = 0; w < 16; ++w) Y += red[(w * 8 + tk) * 16 + col];
      Y = rb(Y);
      const unsigned ord = key >> 16;
      const unsigned rbits = (ord & 0x8000u) ? (ord & 0x7fffu) : (~ord & 0xffffu);
      const float rv = bf2f(rbits);
      const float rsg = rb(1.0f / (1.0f + __expf(-rv)));
      const float y = rb(Y * rsg);
      const float hval = bf2f(hv);
      const float hn = rb(rb(hval - __uint_as_float(stv[0])) / __uint_as_float(stv[1]));
      const float bo = rb(hn + y);
      const int idx = 0xffff - (int)(key & 0xffffu);
      __builtin_amdgcn_raw_buffer_store_b16((short)f2bf(hval + bo), ro, (idx * D_ + c0 + col) * 2, 0, AUX_SC1);
    }
    STAMP(13);
    if (b == 216 && tid == 0) __hip_atomic_store((gu64*)(ws + WS_E), tag, RLX_AGENT);
  }
}

extern "C" void kernel_launch(void* const* d_in, const int* in_sizes, int n_in, void* d_out, int out_size,
                              void* d_ws, size_t ws_size, hipStream_t stream) {
  fused_kernel<<<256, 1024, 0, stream>>>(
      (const u16*)d_in[0], (const u16*)d_in[1], (const u16*)d_in[2], (const u16*)d_in[3], (const u16*)d_in[4],
      (const u16*)d_in[5], (const u16*)d_in[7], (const u16*)d_in[8], (const u16*)d_in[9], (const u16*)d_in[10],
      (u16*)d_out, (char*)d_ws);
}
```

```cpp
#include <hip/hip_runtime.h>
#include <hip/hip_bf16.h>

typedef unsigned short u16;
typedef unsigned long long u64;
using bf16x8 = __attribute__((ext_vector_type(8))) short;
using s16x4 = __attribute__((ext_vector_type(4))) short;
using f32x16 = __attribute__((ext_vector_type(16))) float;
using f32x4 = __attribute__((ext_vector_type(4))) float;
using u32x4 = __attribute__((ext_vector_type(4))) unsigned;
using u32x2 = __attribute__((ext_vector_type(2))) unsigned;
typedef __attribute__((address_space(1))) u64 gu64;

#define DI __device__ __forceinline__
#define RLX_AGENT __ATOMIC_RELAXED, __HIP_MEMORY_SCOPE_AGENT
#define AUX_SC1 16

static constexpr int S_ = 2048;
static constexpr int D_ = 640;
static constexpr int F_ = 1728;

static constexpr unsigned MAT = S_ * D_ * 2;
static constexpr unsigned WS_KVT = 0;
static constexpr unsigned WS_Y2T = MAT;
static constexpr unsigned WS_H = 2 * MAT;
static constexpr unsigned WS_CTL = 3 * MAT;
static constexpr unsigned WS_E = WS_CTL;
static constexpr unsigned WS_STATS = WS_CTL + 4096;
static constexpr unsigned REP_STRIDE = 4352;
static constexpr int R1 = 4;
static constexpr int R2 = 16;
static constexpr int RK = 16;
static constexpr int R4 = 8;
static constexpr unsigned GU_STRIDE = 28160;
static constexpr unsigned WS_F1 = WS_CTL + 32768;
static constexpr unsigned WS_F2 = WS_F1 + R1 * REP_STRIDE;
static constexpr unsigned WS_F3 = WS_F2 + R2 * REP_STRIDE;
static constexpr unsigned WS_F4 = WS_F3 + R2 * REP_STRIDE;
static constexpr unsigned WS_KEYS = WS_F4 + R4 * REP_STRIDE;
static constexpr unsigned WS_GU = WS_KEYS + RK * REP_STRIDE;
static constexpr unsigned WS_BYTES = WS_GU + R4 * GU_STRIDE;

static constexpr int KC_STRIDE = 4160;
static constexpr int KC_BYTES = 8 * KC_STRIDE;
static constexpr int U_PLANE = 2560;
static constexpr int TEAM = 40960;
static constexpr int L_W4 = 122880;
static constexpr int L_W5 = 81920;
static constexpr int L_TB = 143360;
static constexpr int L_CAND = 154880;
static constexpr int L_RED = 155392;
static constexpr int LDS_BYTES = 163840;

DI float bf2f(unsigned x) { return __uint_as_float(x << 16); }
DI float lo2f(unsigned x) { return __uint_as_float(x << 16); }
DI float hi2f(unsigned x) { return __uint_as_float(x & 0xffff0000u); }
DI unsigned f2bf(float x) {
  __bf16 b = (__bf16)x;
  return (unsigned)__builtin_bit_cast(unsigned short, b);
}
DI float rb(float x) { return (float)((__bf16)x); }
DI unsigned pack2(float lo, float hi) { return f2bf(lo) | (f2bf(hi) << 16); }
typedef float f32x2 __attribute__((ext_vector_type(2)));
typedef __bf16 bf16x2_t __attribute__((ext_vector_type(2)));
DI f32x2 unpk(unsigned x) { return (f32x2){lo2f(x), hi2f(x)}; }
DI unsigned pk(f32x2 v) { return __builtin_bit_cast(unsigned, __builtin_convertvector(v, bf16x2_t)); }
DI f32x2 rb2(f32x2 v) { return unpk(pk(v)); }
DI unsigned nosent(unsigned p) {
  if ((p & 0xffffu) == 0xffffu) p ^= 1u;
  if ((p >> 16) == 0xffffu) p ^= 0x10000u;
  return p;
}
DI bool ready4(const u32x4 a) {
  bool ok = true;
#pragma unroll
  for (int d = 0; d < 4; ++d) ok &= ((a[d] & 0xffffu) != 0xffffu) && ((a[d] >> 16) != 0xffffu);
  return ok;
}
DI float fdiv(float a, float b, float rcpb) {
  const float q0 = a * rcpb;
  const float e = __builtin_fmaf(-q0, b, a);
  return __builtin_fmaf(e, rcpb, q0);
}

template <int CTRL>
DI int dpp_i(int v) { return __builtin_amdgcn_update_dpp(0, v, CTRL, 0xF, 0xF, true); }
DI float wave_sum(float v) {
  v += __int_as_float(dpp_i<0xB1>(__float_as_int(v)));
  v += __int_as_float(dpp_i<0x4E>(__float_as_int(v)));
  v += __int_as_float(dpp_i<0x141>(__float_as_int(v)));
  v += __int_as_float(dpp_i<0x140>(__float_as_int(v)));
  float a = __int_as_float(__builtin_amdgcn_readlane(__float_as_int(v), 0));
  float b = __int_as_float(__builtin_amdgcn_readlane(__float_as_int(v), 16));
  float c = __int_as_float(__builtin_amdgcn_readlane(__float_as_int(v), 32));
  float d = __int_as_float(__builtin_amdgcn_readlane(__float_as_int(v), 48));
  return (a + b) + (c + d);
}
DI unsigned umax(unsigned a, unsigned b) { return a > b ? a : b; }
DI unsigned wave_umax(unsigned v) {
  v = umax(v, (unsigned)dpp_i<0xB1>((int)v));
  v = umax(v, (unsigned)dpp_i<0x4E>((int)v));
  v = umax(v, (unsigned)dpp_i<0x141>((int)v));
  v = umax(v, (unsigned)dpp_i<0x140>((int)v));
  unsigned a = (unsigned)__builtin_amdgcn_readlane((int)v, 0);
  unsigned b = (unsigned)__builtin_amdgcn_readlane((int)v, 16);
  unsigned c = (unsigned)__builtin_amdgcn_readlane((int)v, 32);
  unsigned d = (unsigned)__builtin_amdgcn_readlane((int)v, 48);
  return umax(umax(a, b), umax(c, d));
}
typedef __attribute__((ext_vector_type(4))) short v4i16_t;
DI s16x4 tr_read(const char* p) {
  return __builtin_bit_cast(s16x4, __builtin_amdgcn_ds_read_tr16_b64_v4i16(
      (__attribute__((address_space(3))) v4i16_t*)p));
}
DI void glds16(const void* gsrc, unsigned lds_dst) {
  unsigned keep;
  asm volatile("s_mov_b32 %0, m0\n\ts_mov_b32 m0, %2\n\ts_nop 0\n\tglobal_load_lds_dwordx4 %1, off\n\ts_mov_b32 m0, %0"
               : "=&s"(keep) : "v"(gsrc), "s"(lds_dst) : "memory");
}
DI void drain() { asm volatile("s_waitcnt vmcnt(0)" ::: "memory"); }
static constexpr int SPIN_CAP = 1 << 20;
#define STAMP(i) do {} while (0)

template <int MASK>
DI void conv_tiles(const char* kc, const char* us, char* red, int w, int lane) {
  const int rp = lane & 15, g = lane >> 4;
  const char* pa = kc + (rp & 7) * KC_STRIDE + 2 * (2040 - 8 * (rp >> 3) + 8 * g) - 64 * w;
  const char* pb = us + (g >> 1) * U_PLANE + (rp + 16 - w) * 32 + 16 * (g & 1);
  f32x4 acc[4][2];
#pragma unroll
  for (int a = 0; a < 4; ++a) { acc[a][0] = (f32x4){0.f, 0.f, 0.f, 0.f}; acc[a][1] = (f32x4){0.f, 0.f, 0.f, 0.f}; }
#pragma unroll
  for (int mi = 0; mi < 16; ++mi) {
    constexpr int dummy = 0; (void)dummy;
    const int amin = mi >> 2;
    if ((MASK >> amin) == 0) continue;
    const bf16x8 a0 = *(const bf16x8*)(pa - 256 * mi);
    const bf16x8 a1 = *(const bf16x8*)(pa - 256 * mi - 32);
#pragma unroll
    for (int a = 0; a < 4; ++a) {
      if (a >= amin && ((MASK >> a) & 1)) {
        const bf16x8 bb = *(const bf16x8*)(pb + 512 * a - 128 * mi);
        acc[a][0] = __builtin_amdgcn_mfma_f32_16x16x32_bf16(a0, bb, acc[a][0], 0, 0, 0);
        acc[a][1] = __builtin_amdgcn_mfma_f32_16x16x32_bf16(a1, bb, acc[a][1], 0, 0, 0);
      }
    }
  }
  __syncthreads();
  char* rd = red + w * 9216 + rp * 144 + 16 * g;
#pragma unroll
  for (int a = 0; a < 4; ++a) {
    if ((MASK >> a) & 1) {
      *(f32x4*)(rd + a * 16 * 144) = acc[a][0];
      *(f32x4*)(rd + a * 16 * 144 + 64) = acc[a][1];
    }
  }
}

__global__ __launch_bounds__(1024) void fused_kernel(
    const u16* __restrict__ q, const u16* __restrict__ k, const u16* __restrict__ v,
    const u16* __restrict__ shift_k, const u16* __restrict__ ssm_k, const u16* __restrict__ ssm_D,
    const u16* __restrict__ w_r, const u16* __restrict__ w_gate, const u16* __restrict__ w_up,
    const u16* __restrict__ w_down, u16* out, char* ws) {
  __shared__ __attribute__((aligned(16))) char lds[LDS_BYTES];
  asm volatile("" ::"s"(q), "s"(k), "s"(v), "s"(shift_k), "s"(ssm_k), "s"(ssm_D), "s"(w_r), "s"(w_gate), "s"(w_up),
               "s"(w_down), "s"(out), "s"(ws));
  const int b = blockIdx.x;
  const int grp = b & 7, ib = b >> 3;
  const int tid = threadIdx.x, lane = tid & 63;
  const int wave = __builtin_amdgcn_readfirstlane(tid >> 6);
  const int team = wave >> 2, tt = tid & 255;
  const __amdgpu_buffer_rsrc_t rs = __builtin_amdgcn_make_buffer_rsrc(ws, 0, WS_BYTES, 0x00020000);
  const __amdgpu_buffer_rsrc_t ro = __builtin_amdgcn_make_buffer_rsrc(out, 0, MAT, 0x00020000);
  const u32x4 ones = {0xffffffffu, 0xffffffffu, 0xffffffffu, 0xffffffffu};
  STAMP(0);

  const int p1tok = tid / 10, p1chunk = tid - 10 * p1tok;
  const int p1c = 80 * grp + 8 * p1chunk;
  const bool conv_on = team < 3;
  const int chbase = 80 * grp + 5 * (ib >> 1);
  const int odd = ib & 1;
  const int ch = chbase + (team >= 2 ? 2 : (odd ? 3 + team : team));
  const int t0 = (grp * 32 + ib) * 8;
  const unsigned lbase = (unsigned)(size_t)lds;
  u32x4 kown = {0u, 0u, 0u, 0u}, vr = {0u, 0u, 0u, 0u};
  u64 eraw = 0;
  if (wave < 10) {
    const int t = 64 * ib + p1tok;
    kown = *(const u32x4*)(k + (size_t)t * D_ + p1c);
    vr = *(const u32x4*)(v + (size_t)t * D_ + p1c);
    if (wave == 0) eraw = __hip_atomic_load((gu64*)(ws + WS_E), RLX_AGENT);
    *(u32x4*)(lds + (p1tok + 3) * 176 + 16 * p1chunk) = kown;
    asm volatile("" : "+v"(vr), "+v"(eraw));
  } else if (wave == 10) {
    if (lane < 30) {
      const int hr = lane / 10, hc = lane - 10 * hr;
      const int t = 64 * ib - 3 + hr;
      u32x4 kh = {0u, 0u, 0u, 0u};
      if (t >= 0) kh = *(const u32x4*)(k + (size_t)t * D_ + 80 * grp + 8 * hc);
      *(u32x4*)(lds + hr * 176 + 16 * hc) = kh;
    }
    if (b < 216)
      __builtin_amdgcn_raw_buffer_store_b128(ones, rs, WS_GU + (lane >> 3) * GU_STRIDE + ((b & 7) * 27 + (b >> 3)) * 128 + (lane & 7) * 16, 0, AUX_SC1);
  } else if (wave == 11) {
    if (lane < 40) {
      const u32x4 d = *(const u32x4*)(shift_k + (size_t)grp * 320 + 8 * lane);
      *(f32x4*)(lds + 12288 + 32 * lane) = (f32x4){lo2f(d[0]), lo2f(d[2]), hi2f(d[0]), hi2f(d[2])};
      *(f32x4*)(lds + 12288 + 32 * lane + 16) = (f32x4){lo2f(d[1]), lo2f(d[3]), hi2f(d[1]), hi2f(d[3])};
    }
    if (lane < RK) __builtin_amdgcn_raw_buffer_store_b128(ones, rs, WS_KEYS + lane * REP_STRIDE + t0 * 2, 0, AUX_SC1);
  } else if (wave == 15) {
    const char* tp = ws + (size_t)(lane & 31) * (WS_BYTES / 32 & ~15u);
    if (lane >= 32) tp = (const char*)out + (size_t)(lane & 31) * (MAT / 32);
    glds16(tp, lbase + L_RED);
  } else {
    const int chk = chbase + (wave == 14 ? 2 : (odd ? 3 + (wave - 12) : (wave - 12)));
#pragma unroll
    for (int i = 0; i < 4; ++i)
      glds16(ssm_k + (size_t)chk * S_ + 512 * i + 8 * lane, lbase + L_W4 + 4096 * (wave - 12) + 1024 * i);
  }
  __syncthreads();
  unsigned Draw = 0;
  if (wave < 10) {
    u32x4 kr[3];
#pragma unroll
    for (int i = 0; i < 3; ++i) kr[i] = *(const u32x4*)(lds + (p1tok + i) * 176 + 16 * p1chunk);
#pragma unroll
    for (int jp = 0; jp < 4; ++jp) {
      const f32x4 wa = *(const f32x4*)(lds + 12288 + 128 * p1chunk + 32 * jp);
      const f32x4 wb = *(const f32x4*)(lds + 12288 + 128 * p1chunk + 32 * jp + 16);
      const f32x2 w0 = {wa[0], wa[1]}, w1 = {wa[2], wa[3]}, w2 = {wb[0], wb[1]}, w3 = {wb[2], wb[3]};
      f32x2 ks = rb2(unpk(kr[0][jp]) * w3);
      ks = rb2(ks + rb2(unpk(kr[1][jp]) * w2));
      ks = rb2(ks + rb2(unpk(kr[2][jp]) * w1));
      ks = rb2(ks + rb2(unpk(kown[jp]) * w0));
      const unsigned o = pk(ks * unpk(vr[jp]));
      char* dst = lds + L_TB + (8 * p1chunk + 2 * jp) * 128 + 16 * p1chunk + 2 * p1tok;
      *(u16*)dst = (u16)(o & 0xffffu);
      *(u16*)(dst + 128) = (u16)(o >> 16);
    }
  }
  asm volatile("" : "+v"(eraw));
  const u64 tag0 = eraw + 1ull;
  if (tid == 0) { *(u64*)(lds + L_CAND + 384) = tag0; *(unsigned*)(lds + L_CAND + 400) = 0u; }
  if (wave >= 12) drain();
  __syncthreads();
  STAMP(1);
  if (tid < 640) {
    const int row = tid >> 3, piece = tid & 7;
    const u32x4 d = *(const u32x4*)(lds + L_TB + row * 128 + 16 * (row >> 3) + 16 * piece);
    __builtin_amdgcn_raw_buffer_store_b128(d, rs, WS_KVT + ((80 * grp + row) * S_ + 64 * ib + 8 * piece) * 2, 0, AUX_SC1);
  }
  const u64 tag = *(const u64*)(lds + L_CAND + 384);
  char* kc = lds + TEAM * team;
  char* us = kc + KC_BYTES;
  if (conv_on) {
    const u32x4 z4 = {0u, 0u, 0u, 0u};
    u32x4 klo = *(const u32x4*)(lds + L_W4 + 4096 * team + 16 * (tt < 255 ? 254 - tt : 0));
    const u32x4 khi = *(const u32x4*)(lds + L_W4 + 4096 * team + 16 * (255 - tt));
    if (tt >= 255) klo = z4;
    if (tt < 64) *(u32x4*)(us + (tt >> 5) * U_PLANE + 16 * (tt & 31)) = z4;
    if (tt < 32) *(u32x4*)(kc + (tt >> 2) * KC_STRIDE + 16 * (256 + (tt & 3))) = z4;
    const unsigned W[8] = {klo[0], klo[1], klo[2], klo[3], khi[0], khi[1], khi[2], khi[3]};
    unsigned E[7], O[7];
#pragma unroll
    for (int d = 0; d < 7; ++d) {
      E[d] = (W[7 - d] >> 16) | (W[7 - d] << 16);
      O[d] = (W[7 - d] & 0xffffu) | (W[6 - d] & 0xffff0000u);
    }
    char* dst = kc + 16 * tt;
#pragma unroll
    for (int qq = 0; qq < 4; ++qq) {
      *(u32x4*)(dst + (7 - 2 * qq) * KC_STRIDE) = (u32x4){E[qq], E[qq + 1], E[qq + 2], E[qq + 3]};
      *(u32x4*)(dst + (6 - 2 * qq) * KC_STRIDE) = (u32x4){O[qq], O[qq + 1], O[qq + 2], O[qq + 3]};
    }
  }
  if (wave < 12) drain();
  __syncthreads();
  if (tid < R1) __hip_atomic_store((gu64*)(ws + WS_F1 + tid * REP_STRIDE) + grp * 32 + ib, tag, RLX_AGENT);
  STAMP(2);

  unsigned xr[5], wr[5];
  if (wave < 8) {
    const int t = t0 + wave;
#pragma unroll
    for (int i = 0; i < 5; ++i) xr[i] = *(const unsigned*)(q + (size_t)t * D_ + 2 * lane + 128 * i);
#pragma unroll
    for (int i = 0; i < 5; ++i) wr[i] = *(const unsigned*)(w_r + 2 * lane + 128 * i);
  }

  if (wave == 12) {
    const gu64* f = (const gu64*)(ws + WS_F1 + (ib % R1) * REP_STRIDE) + grp * 32 + (lane & 31);
    for (int it = 0; it < SPIN_CAP; ++it) {
      const bool ok = __hip_atomic_load(f, RLX_AGENT) == tag;
      if (__all(ok)) break;
      __builtin_amdgcn_s_sleep(1);
    }
  }
  __syncthreads();
  STAMP(3);
  u32x4 uu = {0u, 0u, 0u, 0u};
  if (conv_on) {
    Draw = ssm_D[ch];
    uu = __builtin_amdgcn_raw_buffer_load_b128(rs, WS_KVT + (ch * S_ + 8 * tt) * 2, 0, AUX_SC1);
    *(u32x4*)(us + ((tt >> 1) & 1) * U_PLANE + ((tt >> 2) + 16) * 32 + 16 * (tt & 1)) = uu;
  }
  __syncthreads();
  STAMP(4);
  if (b < 216) {
    if (wave >= 12) {
      const int f0 = ((b & 7) * 27 + (b >> 3)) * 8;
#pragma unroll
      for (int i = 0; i < 5; ++i) {
        const int p = tt + 256 * i;
        const u16* src = ((p & 1) ? w_up : w_gate) + (size_t)(p >> 1) * F_ + f0;
        glds16(src, lbase + L_W4 + 16 * ((wave - 12) * 64 + 256 * i));
      }
    }
  }
  if (team < 2) conv_tiles<15>(kc, us, kc, wave & 3, lane);
  else if (team == 2) { if (odd) conv_tiles<6>(kc, us, kc, wave & 3, lane); else conv_tiles<9>(kc, us, kc, wave & 3, lane); }
  else __syncthreads();
  STAMP(5);
  __syncthreads();
  const int myal = tt >> 6;
  if (team < 2 || (team == 2 && ((myal == 1 || myal == 2) == (odd != 0)))) {
    const char* red = kc + (tt >> 2) * 144 + (tt & 3) * 32;
    f32x4 s0 = *(const f32x4*)(red), s1 = *(const f32x4*)(red + 16);
#pragma unroll
    for (int w = 1; w < 4; ++w) {
      s0 += *(const f32x4*)(red + w * 9216);
      s1 += *(const f32x4*)(red + w * 9216 + 16);
    }
    const float Dc = bf2f(Draw);
    float o[8];
#pragma unroll
    for (int j = 0; j < 8; ++j) {
      const float acc = j < 4 ? s0[j] : s1[j - 4];
      const float kv = (j & 1) ? hi2f(uu[j >> 1]) : lo2f(uu[j >> 1]);
      o[j] = rb(acc) + rb(kv * Dc);
    }
    const u32x4 st = {pack2(o[0], o[1]), pack2(o[2], o[3]), pack2(o[4], o[5]), pack2(o[6], o[7])};
    __builtin_amdgcn_raw_buffer_store_b128(st, rs, WS_Y2T + (ch * S_ + 8 * tt) * 2, 0, AUX_SC1);
  }
  if (conv_on) drain();
  __syncthreads();
  if (tid < R2) __hip_atomic_store((gu64*)(ws + WS_F2 + tid * REP_STRIDE) + b, tag, RLX_AGENT);
  STAMP(6);
  if (b >= 216 && wave >= 13) {
    const int j5 = b - 216;
    const int c05 = 16 * ((j5 & 7) * 5 + (j5 >> 3));
#pragma unroll
    for (int i = 0; i < 6; ++i) {
      const int pb = ((0 * 6 + i) * 3 + (wave - 13)) * 64;
      const int p = pb + lane;
      glds16(w_down + (size_t)(p >> 1) * D_ + c05 + 8 * (p & 1), lbase + L_W5 + 16 * pb);
    }
  }

  float xf[10], ql[10];
  if (wave < 8) {
#pragma unroll
    for (int i = 0; i < 5; ++i) { xf[2 * i] = lo2f(xr[i]); xf[2 * i + 1] = hi2f(xr[i]); }
    float s = 0.f;
#pragma unroll
    for (int i = 0; i < 10; ++i) s += xf[i];
    const float mean1 = wave_sum(s) * (1.0f / D_);
    float qq = 0.f;
#pragma unroll
    for (int i = 0; i < 10; ++i) { const float d = xf[i] - mean1; qq += d * d; }
    const float var1 = wave_sum(qq) * (1.0f / D_);
    const float m1 = rb(mean1);
    const float sd1 = rb(sqrtf(rb(rb(var1) + 1e-5f)));
    const float rc1 = __builtin_amdgcn_rcpf(sd1);
#pragma unroll
    for (int i = 0; i < 10; ++i) ql[i] = rb(fdiv(rb(xf[i] - m1), sd1, rc1));
    asm volatile("" : "+v"(ql[0]), "+v"(ql[1]), "+v"(ql[2]), "+v"(ql[3]), "+v"(ql[4]), "+v"(ql[5]), "+v"(ql[6]),
                 "+v"(ql[7]), "+v"(ql[8]), "+v"(ql[9]));
  }

  if (wave == 11) {
    const gu64* f = (const gu64*)(ws + WS_F2 + (b % R2) * REP_STRIDE) + lane;
    for (int it = 0; it < SPIN_CAP; ++it) {
      bool ok = true;
#pragma unroll
      for (int i = 0; i < 4; ++i) ok &= __hip_atomic_load(f + 64 * i, RLX_AGENT) == tag;
      if (__all(ok)) break;
      __builtin_amdgcn_s_sleep(1);
    }
  }
  __syncthreads();
  STAMP(7);
  {
    u16* ys = (u16*)(lds + L_TB);
    if (wave < 10) {
      const u32x4 a = __builtin_amdgcn_raw_buffer_load_b128(rs, WS_Y2T + (tid * S_ + t0) * 2, 0, AUX_SC1);
#pragma unroll
      for (int j = 0; j < 8; ++j) ys[j * 648 + tid] = (u16)((j & 1) ? (a[j >> 1] >> 16) : (a[j >> 1] & 0xffffu));
    }
    __syncthreads();
    STAMP(8);
  if (b >= 216 && wave >= 13) {
    const int j5 = b - 216;
    const int c05 = 16 * ((j5 & 7) * 5 + (j5 >> 3));
#pragma unroll
    for (int i = 0; i < 6; ++i) {
      const int pb = ((1 * 6 + i) * 3 + (wave - 13)) * 64;
      const int p = pb + lane;
      glds16(w_down + (size_t)(p >> 1) * D_ + c05 + 8 * (p & 1), lbase + L_W5 + 16 * pb);
    }
  }
    float hf[10], hn[10];
    float m2 = 0.f, sd2 = 1.f;
    if (wave < 8) {
#pragma unroll
      for (int i = 0; i < 5; ++i) {
        const unsigned yy = *(const unsigned*)&ys[wave * 648 + 2 * lane + 128 * i];
        hf[2 * i] = rb(xf[2 * i] + rb(ql[2 * i] * lo2f(yy)));
        hf[2 * i + 1] = rb(xf[2 * i + 1] + rb(ql[2 * i + 1] * hi2f(yy)));
      }
      float s = 0.f;
#pragma unroll
      for (int i = 0; i < 10; ++i) s += hf[i];
      const float mean2 = wave_sum(s) * (1.0f / D_);
      float qq = 0.f;
#pragma unroll
      for (int i = 0; i < 10; ++i) { const float d = hf[i] - mean2; qq += d * d; }
      const float var2 = wave_sum(qq) * (1.0f / D_);
      m2 = rb(mean2);
      sd2 = rb(sqrtf(rb(rb(var2) + 1e-5f)));
      const float rc2 = __builtin_amdgcn_rcpf(sd2);
      float dot = 0.f;
#pragma unroll
      for (int i = 0; i < 5; ++i) {
        hn[2 * i] = rb(fdiv(rb(hf[2 * i] - m2), sd2, rc2));
        hn[2 * i + 1] = rb(fdiv(rb(hf[2 * i + 1] - m2), sd2, rc2));
        dot += hn[2 * i] * lo2f(wr[i]) + hn[2 * i + 1] * hi2f(wr[i]);
      }
      const float rr = wave_sum(dot);
      unsigned rbits = f2bf(rr);
      if (rbits == 0xffffu) rbits = 0xfffeu;
      if (lane == 0) ((u16*)(lds + L_CAND))[wave] = (u16)rbits;
    }
    __syncthreads();
  if (b >= 216 && wave >= 13) {
    const int j5 = b - 216;
    const int c05 = 16 * ((j5 & 7) * 5 + (j5 >> 3));
#pragma unroll
    for (int i = 0; i < 6; ++i) {
      const int pb = ((2 * 6 + i) * 3 + (wave - 13)) * 64;
      const int p = pb + lane;
      glds16(w_down + (size_t)(p >> 1) * D_ + c05 + 8 * (p & 1), lbase + L_W5 + 16 * pb);
    }
  }
    if (wave == 8 && lane < RK) {
      const u32x4 kq = *(const u32x4*)(lds + L_CAND);
      __builtin_amdgcn_raw_buffer_store_b128(kq, rs, WS_KEYS + lane * REP_STRIDE + t0 * 2, 0, AUX_SC1);
    }
    if (wave < 8) {
      const int t = t0 + wave;
      unsigned* stg = (unsigned*)(lds + wave * 2560);
#pragma unroll
      for (int i = 0; i < 5; ++i) {
        stg[lane + 64 * i] = pack2(hf[2 * i] + hn[2 * i], hf[2 * i + 1] + hn[2 * i + 1]);
        stg[320 + lane + 64 * i] = pack2(hf[2 * i], hf[2 * i + 1]);
      }
      __builtin_amdgcn_fence(__ATOMIC_RELEASE, "wavefront");
      __builtin_amdgcn_wave_barrier();
      __builtin_amdgcn_fence(__ATOMIC_ACQUIRE, "wavefront");
      const u32x4 h0 = *(const u32x4*)(stg + 320 + 4 * lane);
      __builtin_amdgcn_raw_buffer_store_b128(h0, rs, WS_H + (t * D_ + 8 * lane) * 2, 0, AUX_SC1);
      if (lane < 16) {
        const u32x4 h1 = *(const u32x4*)(stg + 320 + 256 + 4 * lane);
        __builtin_amdgcn_raw_buffer_store_b128(h1, rs, WS_H + (t * D_ + 512 + 8 * lane) * 2, 0, AUX_SC1);
      }
      if (lane == 0) {
        const u32x2 sv = {__float_as_uint(m2), __float_as_uint(sd2)};
        __builtin_amdgcn_raw_buffer_store_b64(sv, rs, WS_STATS + t * 8, 0, AUX_SC1);
      }
    }
    drain();
    if (wave < 8) {
      unsigned old = 0;
      if (lane == 0) old = atomicAdd((unsigned*)(lds + L_CAND + 400), 1u);
      old = __builtin_amdgcn_readfirstlane(old);
      if (old == 7u && lane < R2) __hip_atomic_store((gu64*)(ws + WS_F3 + lane * REP_STRIDE) + b, tag, RLX_AGENT);
    }
  }
  STAMP(9);

  unsigned* cand = (unsigned*)(lds + L_CAND);
  unsigned kk[8];
  const bool tkw = (wave >= 8 && wave < 12);
  const int tkt = tid - 512;
  if (tkw) {
    u32x4 gq = ones;
    __builtin_amdgcn_s_sleep(24);
    for (int it = 0; it < SPIN_CAP; ++it) {
      gq = __builtin_amdgcn_raw_buffer_load_b128(rs, WS_KEYS + (b % RK) * REP_STRIDE + tkt * 16, 0, AUX_SC1);
      if (__all(ready4(gq))) break;
      __builtin_amdgcn_s_sleep(10);
    }
#pragma unroll
    for (int j = 0; j < 8; ++j) {
      unsigned x = (j & 1) ? (gq[j >> 1] >> 16) : (gq[j >> 1] & 0xffffu);
      x = (x & 0x8000u) ? (~x & 0xffffu) : (x | 0x8000u);
      kk[j] = (x << 16) | (unsigned)(0xffff - 8 * tkt - j);
    }
    unsigned m = kk[0];
#pragma unroll
    for (int j = 1; j < 8; ++j) m = umax(m, kk[j]);
    const unsigned M1 = wave_umax(m);
    m = 0;
#pragma unroll
    for (int j = 0; j < 8; ++j) m = umax(m, kk[j] == M1 ? 0u : kk[j]);
    const unsigned M2 = wave_umax(m);
    if (lane == 0) { cand[64 + wave - 8] = M2; if (wave == 8) cand[68] = 0; }
  }
  __syncthreads();
  STAMP(10);
  if (tkw) {
    const unsigned th = min(min(cand[64], cand[65]), min(cand[66], cand[67]));
#pragma unroll
    for (int j = 0; j < 8; ++j) {
      if (kk[j] >= th) {
        const unsigned pos = atomicAdd(&cand[68], 1u);
        if (pos < 64) cand[pos] = kk[j];
      }
    }
  }
  __syncthreads();
  if (wave == 12) {
    const gu64* f = (const gu64*)(ws + WS_F3 + (b % R2) * REP_STRIDE) + lane;
    for (int it = 0; it < SPIN_CAP; ++it) {
      bool ok = true;
#pragma unroll
      for (int i = 0; i < 4; ++i) ok &= __hip_atomic_load(f + 64 * i, RLX_AGENT) == tag;
      if (__all(ok)) break;
      __builtin_amdgcn_s_sleep(1);
    }
  }
  const unsigned ncand = cand[68];
  if (ncand <= 64) {
    if (wave == 0) {
      const unsigned mine = (lane < (int)ncand) ? cand[lane] : 0u;
      unsigned rank = 0;
      for (unsigned i = 0; i < ncand; ++i) rank += (cand[i] > mine) ? 1u : 0u;
      if (lane < (int)ncand && rank < 8) cand[72 + rank] = mine;
    }
  } else {
    for (int rnd = 0; rnd < 8; ++rnd) {
      __syncthreads();
      if (tkw) {
        unsigned m = kk[0];
#pragma unroll
        for (int j = 1; j < 8; ++j) m = umax(m, kk[j]);
        const unsigned M = wave_umax(m);
        if (lane == 0) cand[64 + wave - 8] = M;
      }
      __syncthreads();
      const unsigned M = umax(umax(cand[64], cand[65]), umax(cand[66], cand[67]));
      if (tkw) {
#pragma unroll
        for (int j = 0; j < 8; ++j) kk[j] = (kk[j] == M) ? 0u : kk[j];
      }
      if (tid == 0) cand[72 + rnd] = M;
    }
  }
  __syncthreads();
  STAMP(11);
  if (wave >= 10) {
    for (int rr = wave - 10; rr < 8; rr += 6) {
      const int t = t0 + rr;
      bool routed = false;
#pragma unroll
      for (int j = 0; j < 8; ++j) routed |= (0xffff - (int)(cand[72 + j] & 0xffffu)) == t;
      if (!routed) {
        const unsigned* stg = (const unsigned*)(lds + rr * 2560);
        __builtin_amdgcn_raw_buffer_store_b128(*(const u32x4*)(stg + 4 * lane), ro, (t * D_ + 8 * lane) * 2, 0, AUX_SC1);
        if (lane < 16) __builtin_amdgcn_raw_buffer_store_b128(*(const u32x4*)(stg + 256 + 4 * lane), ro, (t * D_ + 512 + 8 * lane) * 2, 0, AUX_SC1);
      }
    }
  }
  const int tok = lane & 15, g = lane >> 4, i16 = lane & 15;
  float* red = (float*)(lds + L_RED);
  if (b < 216) {
    const int slab = (b & 7) * 27 + (b >> 3);
    const int f0 = slab * 8;
    if (wave < 10) {
      const unsigned mykey = cand[72 + (tok & 7)];
      const int myidx = 0xffff - (int)(mykey & 0xffffu);
      const u32x2 stv = __builtin_amdgcn_raw_buffer_load_b64(rs, WS_STATS + myidx * 8, 0, AUX_SC1);
      u32x4 hraw[2];
#pragma unroll
      for (int i = 0; i < 2; ++i)
        hraw[i] = __builtin_amdgcn_raw_buffer_load_b128(rs, WS_H + (myidx * D_ + 32 * (2 * wave + i) + 8 * g) * 2, 0, AUX_SC1);
      const float sm = __uint_as_float(stv[0]), ss = __uint_as_float(stv[1]);
      const float rcs = __builtin_amdgcn_rcpf(ss);
      f32x4 acc = {0.f, 0.f, 0.f, 0.f};
#pragma unroll
      for (int i = 0; i < 2; ++i) {
        const int k0 = 32 * (2 * wave + i);
        u32x4 HN;
#pragma unroll
        for (int d = 0; d < 4; ++d) {
          const float a = fdiv(rb(lo2f(hraw[i][d]) - sm), ss, rcs);
          const float bb = fdiv(rb(hi2f(hraw[i][d]) - sm), ss, rcs);
          HN[d] = pack2(a, bb);
        }
        const char* p = lds + L_W4 + (k0 + 8 * g + (i16 >> 2)) * 32 + 8 * (i16 & 3);
        const s16x4 blo = tr_read(p);
        const s16x4 bhi = tr_read(p + 4 * 32);
        const bf16x8 bfrag = __builtin_shufflevector(blo, bhi, 0, 1, 2, 3, 4, 5, 6, 7);
        acc = __builtin_amdgcn_mfma_f32_16x16x32_bf16(__builtin_bit_cast(bf16x8, HN), bfrag, acc, 0, 0, 0);
      }
      if (g < 2) {
#pragma unroll
        for (int j = 0; j < 4; ++j) red[(wave * 8 + 4 * g + j) * 16 + i16] = acc[j];
      }
    }
    __syncthreads();
    STAMP(12);
    if (wave == 0) {
      const int tk = lane >> 3, col = lane & 7;
      float G = 0.f, U = 0.f;
#pragma unroll
      for (int w = 0; w < 10; ++w) {
        G += red[(w * 8 + tk) * 16 + col];
        U += red[(w * 8 + tk) * 16 + col + 8];
      }
      G = rb(G); U = rb(U);
      const float gg = rb(G / (1.0f + __expf(-G)));
      u16* stg = (u16*)(lds + L_CAND);
      unsigned gub = f2bf(gg * U);
      if (gub == 0xffffu) gub = 0xfffeu;
      stg[lane] = (u16)gub;
      __builtin_amdgcn_fence(__ATOMIC_RELEASE, "wavefront");
      __builtin_amdgcn_wave_barrier();
      __builtin_amdgcn_fence(__ATOMIC_ACQUIRE, "wavefront");
      {
        const u32x4 o = *(const u32x4*)(stg + 8 * (lane & 7));
        __builtin_amdgcn_raw_buffer_store_b128(o, rs, WS_GU + (lane >> 3) * GU_STRIDE + slab * 128 + (lane & 7) * 16, 0, AUX_SC1);
      }
      STAMP(13);
    }
  } else {
    const int j = b - 216;
    const int c0 = 16 * ((j & 7) * 5 + (j >> 3));
    unsigned key = 0, hv = 0;
    u32x2 stv = {0u, 0u};
    if (wave < 2) {
      const int tk = tid >> 4;
      key = cand[72 + tk];
      const int idx = 0xffff - (int)(key & 0xffffu);
      hv = (unsigned)(u16)__builtin_amdgcn_raw_buffer_load_b16(rs, WS_H + (idx * D_ + c0 + (tid & 15)) * 2, 0, AUX_SC1);
      stv = __builtin_amdgcn_raw_buffer_load_b64(rs, WS_STATS + idx * 8, 0, AUX_SC1);
    }
    {
      u32x4 af[4];
      __builtin_amdgcn_s_sleep(40);
      for (int it = 0; it < SPIN_CAP; ++it) {
        bool ok = true;
#pragma unroll
        for (int i = 0; i < 4; ++i) {
          const int ks = wave + 16 * i;
          const int ksc = ks < 54 ? ks : 0;
          af[i] = (u32x4){0u, 0u, 0u, 0u};
          if (tok < 8) af[i] = __builtin_amdgcn_raw_buffer_load_b128(rs, WS_GU + (j % R4) * GU_STRIDE + (4 * ksc + g) * 128 + tok * 16, 0, AUX_SC1);
        }
#pragma unroll
        for (int i = 0; i < 4; ++i) ok &= ready4(af[i]);
        if (__all(ok)) break;
        __builtin_amdgcn_s_sleep(2);
      }
      STAMP(12);
      f32x4 acc = {0.f, 0.f, 0.f, 0.f};
#pragma unroll
      for (int i = 0; i < 4; ++i) {
        const int ks = wave + 16 * i;
        if (ks < 54) {
          const int k0 = 32 * ks;
          const char* p = lds + L_W5 + (k0 + 8 * g + (i16 >> 2)) * 32 + 8 * (i16 & 3);
          const s16x4 blo = tr_read(p);
          const s16x4 bhi = tr_read(p + 4 * 32);
          const bf16x8 bfrag = __builtin_shufflevector(blo, bhi, 0, 1, 2, 3, 4, 5, 6, 7);
          acc = __builtin_amdgcn_mfma_f32_16x16x32_bf16(__builtin_bit_cast(bf16x8, af[i]), bfrag, acc, 0, 0, 0);
        }
      }
      if (g < 2) {
#pragma unroll
        for (int jj = 0; jj < 4; ++jj) red[(wave * 8 + 4 * g + jj) * 16 + i16] = acc[jj];
      }
    }
    __syncthreads();
    if (wave < 2) {
      const int tk = tid >> 4, col = tid & 15;
      float Y = 0.f;
#pragma unroll
      for (int w = 0; w < 16; ++w) Y += red[(w * 8 + tk) * 16 + col];
      Y = rb(Y);
      const unsigned ord = key >> 16;
      const unsigned rbits = (ord & 0x8000u) ? (ord & 0x7fffu) : (~ord & 0xffffu);
      const float rv = bf2f(rbits);
      const float rsg = rb(1.0f / (1.0f + __expf(-rv)));
      const float y = rb(Y * rsg);
      const float hval = bf2f(hv);
      const float hn = rb(rb(hval - __uint_as_float(stv[0])) / __uint_as_float(stv[1]));
      const float bo = rb(hn + y);
      const int idx = 0xffff - (int)(key & 0xffffu);
      __builtin_amdgcn_raw_buffer_store_b16((short)f2bf(hval + bo), ro, (idx * D_ + c0 + col) * 2, 0, AUX_SC1);
    }
    STAMP(13);
    if (b == 216 && tid == 0) __hip_atomic_store((gu64*)(ws + WS_E), tag, RLX_AGENT);
  }
}

extern "C" void kernel_launch(void* const* d_in, const int* in_sizes, int n_in, void* d_out, int out_size,
                              void* d_ws, size_t ws_size, hipStream_t stream) {
  fused_kernel<<<256, 1024, 0, stream>>>(
      (const u16*)d_in[0], (const u16*)d_in[1], (const u16*)d_in[2], (const u16*)d_in[3], (const u16*)d_in[4],
      (const u16*)d_in[5], (const u16*)d_in[7], (const u16*)d_in[8], (const u16*)d_in[9], (const u16*)d_in[10],
      (u16*)d_out, (char*)d_ws);
}
```

```cpp
#include <hip/hip_runtime.h>
#include <hip/hip_bf16.h>

typedef unsigned short u16;
typedef unsigned long long u64;
using bf16x8 = __attribute__((ext_vector_type(8))) short;
using s16x4 = __attribute__((ext_vector_type(4))) short;
using f32x16 = __attribute__((ext_vector_type(16))) float;
using f32x4 = __attribute__((ext_vector_type(4))) float;
using u32x4 = __attribute__((ext_vector_type(4))) unsigned;
using u32x2 = __attribute__((ext_vector_type(2))) unsigned;
typedef __attribute__((address_space(1))) u64 gu64;

#define DI __device__ __forceinline__
#define RLX_AGENT __ATOMIC_RELAXED, __HIP_MEMORY_SCOPE_AGENT
#define AUX_SC1 16

static constexpr int S_ = 2048;
static constexpr int D_ = 640;
static constexpr int F_ = 1728;

static constexpr unsigned MAT = S_ * D_ * 2;
static constexpr unsigned WS_KVT = 0;
static constexpr unsigned WS_Y2T = MAT;
static constexpr unsigned WS_H = 2 * MAT;
static constexpr unsigned WS_CTL = 3 * MAT;
static constexpr unsigned WS_E = WS_CTL;
static constexpr unsigned WS_STATS = WS_CTL + 4096;
static constexpr unsigned REP_STRIDE = 4352;
static constexpr int R1 = 16;
static constexpr int R2 = 16;
static constexpr int RK = 16;
static constexpr int R4 = 8;
static constexpr unsigned GU_STRIDE = 28160;
static constexpr unsigned WS_F1 = WS_CTL + 32768;
static constexpr unsigned WS_F2 = WS_F1 + R1 * REP_STRIDE;
static constexpr unsigned WS_F3 = WS_F2 + R2 * REP_STRIDE;
static constexpr unsigned WS_F4 = WS_F3 + R2 * REP_STRIDE;
static constexpr unsigned WS_KEYS = WS_F4 + R4 * REP_STRIDE;
static constexpr unsigned WS_GU = WS_KEYS + RK * REP_STRIDE;
static constexpr unsigned WS_BYTES = WS_GU + R4 * GU_STRIDE;

static constexpr int KC_STRIDE = 4160;
static constexpr int KC_BYTES = 8 * KC_STRIDE;
static constexpr int U_PLANE = 2560;
static constexpr int TEAM = 40960;
static constexpr int L_W4 = 122880;
static constexpr int L_W5 = 81920;
static constexpr int L_TB = 143360;
static constexpr int L_CAND = 154880;
static constexpr int L_RED = 155392;
static constexpr int LDS_BYTES = 163840;

DI float bf2f(unsigned x) { return __uint_as_float(x << 16); }
DI float lo2f(unsigned x) { return __uint_as_float(x << 16); }
DI float hi2f(unsigned x) { return __uint_as_float(x & 0xffff0000u); }
DI unsigned f2bf(float x) {
  __bf16 b = (__bf16)x;
  return (unsigned)__builtin_bit_cast(unsigned short, b);
}
DI float rb(float x) { return (float)((__bf16)x); }
DI unsigned pack2(float lo, float hi) { return f2bf(lo) | (f2bf(hi) << 16); }
typedef float f32x2 __attribute__((ext_vector_type(2)));
typedef __bf16 bf16x2_t __attribute__((ext_vector_type(2)));
DI f32x2 unpk(unsigned x) { return (f32x2){lo2f(x), hi2f(x)}; }
DI unsigned pk(f32x2 v) { return __builtin_bit_cast(unsigned, __builtin_convertvector(v, bf16x2_t)); }
DI f32x2 rb2(f32x2 v) { return unpk(pk(v)); }
DI unsigned nosent(unsigned p) {
  if ((p & 0xffffu) == 0xffffu) p ^= 1u;
  if ((p >> 16) == 0xffffu) p ^= 0x10000u;
  return p;
}
DI bool ready4(const u32x4 a) {
  bool ok = true;
#pragma unroll
  for (int d = 0; d < 4; ++d) ok &= ((a[d] & 0xffffu) != 0xffffu) && ((a[d] >> 16) != 0xffffu);
  return ok;
}
DI float fdiv(float a, float b, float rcpb) {
  const float q0 = a * rcpb;
  const float e = __builtin_fmaf(-q0, b, a);
  return __builtin_fmaf(e, rcpb, q0);
}

DI f32x2 fdiv2(f32x2 a, float b, float rcpb) {
  const f32x2 q0 = a * rcpb;
  const f32x2 e = __builtin_elementwise_fma(-q0, (f32x2){b, b}, a);
  return __builtin_elementwise_fma(e, (f32x2){rcpb, rcpb}, q0);
}

template <int CTRL>
DI int dpp_i(int v) { return __builtin_amdgcn_update_dpp(0, v, CTRL, 0xF, 0xF, true); }
DI float wave_sum(float v) {
  v += __int_as_float(dpp_i<0xB1>(__float_as_int(v)));
  v += __int_as_float(dpp_i<0x4E>(__float_as_int(v)));
  v += __int_as_float(dpp_i<0x141>(__float_as_int(v)));
  v += __int_as_float(dpp_i<0x140>(__float_as_int(v)));
  float a = __int_as_float(__builtin_amdgcn_readlane(__float_as_int(v), 0));
  float b = __int_as_float(__builtin_amdgcn_readlane(__float_as_int(v), 16));
  float c = __int_as_float(__builtin_amdgcn_readlane(__float_as_int(v), 32));
  float d = __int_as_float(__builtin_amdgcn_readlane(__float_as_int(v), 48));
  return (a + b) + (c + d);
}
DI unsigned umax(unsigned a, unsigned b) { return a > b ? a : b; }
DI unsigned wave_umax(unsigned v) {
  v = umax(v, (unsigned)dpp_i<0xB1>((int)v));
  v = umax(v, (unsigned)dpp_i<0x4E>((int)v));
  v = umax(v, (unsigned)dpp_i<0x141>((int)v));
  v = umax(v, (unsigned)dpp_i<0x140>((int)v));
  unsigned a = (unsigned)__builtin_amdgcn_readlane((int)v, 0);
  unsigned b = (unsigned)__builtin_amdgcn_readlane((int)v, 16);
  unsigned c = (unsigned)__builtin_amdgcn_readlane((int)v, 32);
  unsigned d = (unsigned)__builtin_amdgcn_readlane((int)v, 48);
  return umax(umax(a, b), umax(c, d));
}
typedef __attribute__((ext_vector_type(4))) short v4i16_t;
DI s16x4 tr_read(const char* p) {
  return __builtin_bit_cast(s16x4, __builtin_amdgcn_ds_read_tr16_b64_v4i16(
      (__attribute__((address_space(3))) v4i16_t*)p));
}
DI void glds16(const void* gsrc, unsigned lds_dst) {
  unsigned keep;
  asm volatile("s_mov_b32 %0, m0\n\ts_mov_b32 m0, %2\n\ts_nop 0\n\tglobal_load_lds_dwordx4 %1, off\n\ts_mov_b32 m0, %0"
               : "=&s"(keep) : "v"(gsrc), "s"(lds_dst) : "memory");
}
DI void drain() { asm volatile("s_waitcnt vmcnt(0)" ::: "memory"); }
static constexpr int SPIN_CAP = 1 << 20;
#define YSLEEP 8
#define YPER 2
#define STAMP(i) do {} while (0)

template <int MASK>
DI void conv_tiles(const char* kc, const char* us, char* red, int w, int lane) {
  const int rp = lane & 15, g = lane >> 4;
  const char* pa = kc + (rp & 7) * KC_STRIDE + 2 * (2040 - 8 * (rp >> 3) + 8 * g) - 64 * w;
  const char* pb = us + (g >> 1) * U_PLANE + (rp + 16 - w) * 32 + 16 * (g & 1);
  f32x4 acc[4][2];
#pragma unroll
  for (int a = 0; a < 4; ++a) { acc[a][0] = (f32x4){0.f, 0.f, 0.f, 0.f}; acc[a][1] = (f32x4){0.f, 0.f, 0.f, 0.f}; }
#pragma unroll
  for (int mi = 0; mi < 16; ++mi) {
    constexpr int dummy = 0; (void)dummy;
    const int amin = mi >> 2;
    if ((MASK >> amin) == 0) continue;
    const bf16x8 a0 = *(const bf16x8*)(pa - 256 * mi);
    const bf16x8 a1 = *(const bf16x8*)(pa - 256 * mi - 32);
#pragma unroll
    for (int a = 0; a < 4; ++a) {
      if (a >= amin && ((MASK >> a) & 1)) {
        const bf16x8 bb = *(const bf16x8*)(pb + 512 * a - 128 * mi);
        acc[a][0] = __builtin_amdgcn_mfma_f32_16x16x32_bf16(a0, bb, acc[a][0], 0, 0, 0);
        acc[a][1] = __builtin_amdgcn_mfma_f32_16x16x32_bf16(a1, bb, acc[a][1], 0, 0, 0);
      }
    }
  }
  __syncthreads();
  char* rd = red + w * 9216 + rp * 144 + 16 * g;
#pragma unroll
  for (int a = 0; a < 4; ++a) {
    if ((MASK >> a) & 1) {
      *(f32x4*)(rd + a * 16 * 144) = acc[a][0];
      *(f32x4*)(rd + a * 16 * 144 + 64) = acc[a][1];
    }
  }
}

__global__ __launch_bounds__(1024) void fused_kernel(
    const u16* __restrict__ q, const u16* __restrict__ k, const u16* __restrict__ v,
    const u16* __restrict__ shift_k, const u16* __restrict__ ssm_k, const u16* __restrict__ ssm_D,
    const u16* __restrict__ w_r, const u16* __restrict__ w_gate, const u16* __restrict__ w_up,
    const u16* __restrict__ w_down, u16* out, char* ws) {
  __shared__ __attribute__((aligned(16))) char lds[LDS_BYTES];
  asm volatile("" ::"s"(q), "s"(k), "s"(v), "s"(shift_k), "s"(ssm_k), "s"(ssm_D), "s"(w_r), "s"(w_gate), "s"(w_up),
               "s"(w_down), "s"(out), "s"(ws));
  const int b = blockIdx.x;
  const int grp = b & 7, ib = b >> 3;
  const int tid = threadIdx.x, lane = tid & 63;
  const int wave = __builtin_amdgcn_readfirstlane(tid >> 6);
  const int team = wave >> 2, tt = tid & 255;
  const __amdgpu_buffer_rsrc_t rs = __builtin_amdgcn_make_buffer_rsrc(ws, 0, WS_BYTES, 0x00020000);
  const __amdgpu_buffer_rsrc_t ro = __builtin_amdgcn_make_buffer_rsrc(out, 0, MAT, 0x00020000);
  const u32x4 ones = {0xffffffffu, 0xffffffffu, 0xffffffffu, 0xffffffffu};
  STAMP(0);

  const int p1tok = tid / 10, p1chunk = tid - 10 * p1tok;
  const int p1c = 80 * grp + 8 * p1chunk;
  const bool conv_on = team < 3;
  const int chbase = 80 * grp + 5 * (ib >> 1);
  const int odd = ib & 1;
  const int ch = chbase + (team >= 2 ? 2 : (odd ? 3 + team : team));
  const int t0 = (grp * 32 + ib) * 8;
  const unsigned lbase = (unsigned)(size_t)lds;
  u32x4 kown = {0u, 0u, 0u, 0u}, vr = {0u, 0u, 0u, 0u};
  u64 eraw = 0;
  if (wave < 10) {
    const int t = 64 * ib + p1tok;
    kown = *(const u32x4*)(k + (size_t)t * D_ + p1c);
    vr = *(const u32x4*)(v + (size_t)t * D_ + p1c);
    if (wave == 0) eraw = __hip_atomic_load((gu64*)(ws + WS_E), RLX_AGENT);
    *(u32x4*)(lds + (p1tok + 3) * 176 + 16 * p1chunk) = kown;
    asm volatile("" : "+v"(vr), "+v"(eraw));
  } else if (wave == 10) {
    if (lane < 30) {
      const int hr = lane / 10, hc = lane - 10 * hr;
      const int t = 64 * ib - 3 + hr;
      u32x4 kh = {0u, 0u, 0u, 0u};
      if (t >= 0) kh = *(const u32x4*)(k + (size_t)t * D_ + 80 * grp + 8 * hc);
      *(u32x4*)(lds + hr * 176 + 16 * hc) = kh;
    }
    if (b < 216)
      __builtin_amdgcn_raw_buffer_store_b128(ones, rs, WS_GU + (lane >> 3) * GU_STRIDE + ((b & 7) * 27 + (b >> 3)) * 128 + (lane & 7) * 16, 0, AUX_SC1);
  } else if (wave == 11) {
    if (lane < 40) {
      const u32x4 d = *(const u32x4*)(shift_k + (size_t)grp * 320 + 8 * lane);
      *(f32x4*)(lds + 12288 + 32 * lane) = (f32x4){lo2f(d[0]), lo2f(d[2]), hi2f(d[0]), hi2f(d[2])};
      *(f32x4*)(lds + 12288 + 32 * lane + 16) = (f32x4){lo2f(d[1]), lo2f(d[3]), hi2f(d[1]), hi2f(d[3])};
    }
    if (lane < RK) __builtin_amdgcn_raw_buffer_store_b128(ones, rs, WS_KEYS + lane * REP_STRIDE + t0 * 2, 0, AUX_SC1);
  } else if (wave == 15) {
    const char* tp = ws + (size_t)(lane & 31) * (WS_BYTES / 32 & ~15u);
    if (lane >= 32) tp = (const char*)out + (size_t)(lane & 31) * (MAT / 32);
    glds16(tp, lbase + L_RED);
  } else {
    const int chk = chbase + (wave == 14 ? 2 : (odd ? 3 + (wave - 12) : (wave - 12)));
#pragma unroll
    for (int i = 0; i < 4; ++i)
      glds16(ssm_k + (size_t)chk * S_ + 512 * i + 8 * lane, lbase + L_W4 + 4096 * (wave - 12) + 1024 * i);
  }
  __syncthreads();
  unsigned Draw = 0;
  if (wave < 10) {
    u32x4 kr[3];
#pragma unroll
    for (int i = 0; i < 3; ++i) kr[i] = *(const u32x4*)(lds + (p1tok + i) * 176 + 16 * p1chunk);
#pragma unroll
    for (int jp = 0; jp < 4; ++jp) {
      const f32x4 wa = *(const f32x4*)(lds + 12288 + 128 * p1chunk + 32 * jp);
      const f32x4 wb = *(const f32x4*)(lds + 12288 + 128 * p1chunk + 32 * jp + 16);
      const f32x2 w0 = {wa[0], wa[1]}, w1 = {wa[2], wa[3]}, w2 = {wb[0], wb[1]}, w3 = {wb[2], wb[3]};
      f32x2 ks = rb2(unpk(kr[0][jp]) * w3);
      ks = rb2(ks + rb2(unpk(kr[1][jp]) * w2));
      ks = rb2(ks + rb2(unpk(kr[2][jp]) * w1));
      ks = rb2(ks + rb2(unpk(kown[jp]) * w0));
      const unsigned o = pk(ks * unpk(vr[jp]));
      char* dst = lds + L_TB + (8 * p1chunk + 2 * jp) * 128 + 16 * p1chunk + 2 * p1tok;
      *(u16*)dst = (u16)(o & 0xffffu);
      *(u16*)(dst + 128) = (u16)(o >> 16);
    }
  }
  asm volatile("" : "+v"(eraw));
  const u64 tag0 = eraw + 1ull;
  if (tid == 0) { *(u64*)(lds + L_CAND + 384) = tag0; *(unsigned*)(lds + L_CAND + 400) = 0u; }
  if (wave >= 12) drain();
  __syncthreads();
  STAMP(1);
  if (tid < 640) {
    const int row = tid >> 3, piece = tid & 7;
    const u32x4 d = *(const u32x4*)(lds + L_TB + row * 128 + 16 * (row >> 3) + 16 * piece);
    __builtin_amdgcn_raw_buffer_store_b128(d, rs, WS_KVT + ((80 * grp + row) * S_ + 64 * ib + 8 * piece) * 2, 0, AUX_SC1);
  }
  const int myal = tt >> 6;
  const bool ywr = team < 2 || (team == 2 && ((myal == 1 || myal == 2) == (odd != 0)));
  if (ywr) __builtin_amdgcn_raw_buffer_store_b128(ones, rs, WS_Y2T + (ch * S_ + 8 * tt) * 2, 0, AUX_SC1);
  const u64 tag = *(const u64*)(lds + L_CAND + 384);
  char* kc = lds + TEAM * team;
  char* us = kc + KC_BYTES;
  if (conv_on) {
    const u32x4 z4 = {0u, 0u, 0u, 0u};
    u32x4 klo = *(const u32x4*)(lds + L_W4 + 4096 * team + 16 * (tt < 255 ? 254 - tt : 0));
    const u32x4 khi = *(const u32x4*)(lds + L_W4 + 4096 * team + 16 * (255 - tt));
    if (tt >= 255) klo = z4;
    if (tt < 64) *(u32x4*)(us + (tt >> 5) * U_PLANE + 16 * (tt & 31)) = z4;
    if (tt < 32) *(u32x4*)(kc + (tt >> 2) * KC_STRIDE + 16 * (256 + (tt & 3))) = z4;
    const unsigned W[8] = {klo[0], klo[1], klo[2], klo[3], khi[0], khi[1], khi[2], khi[3]};
    unsigned E[7], O[7];
#pragma unroll
    for (int d = 0; d < 7; ++d) {
      E[d] = (W[7 - d] >> 16) | (W[7 - d] << 16);
      O[d] = (W[7 - d] & 0xffffu) | (W[6 - d] & 0xffff0000u);
    }
    char* dst = kc + 16 * tt;
#pragma unroll
    for (int qq = 0; qq < 4; ++qq) {
      *(u32x4*)(dst + (7 - 2 * qq) * KC_STRIDE) = (u32x4){E[qq], E[qq + 1], E[qq + 2], E[qq + 3]};
      *(u32x4*)(dst + (6 - 2 * qq) * KC_STRIDE) = (u32x4){O[qq], O[qq + 1], O[qq + 2], O[qq + 3]};
    }
  }
  if (wave < 12) drain();
  __syncthreads();
  if (tid < R1) __hip_atomic_store((gu64*)(ws + WS_F1 + tid * REP_STRIDE) + b, tag, RLX_AGENT);
  STAMP(2);

  unsigned xr[5], wr[5];
  if (wave < 8) {
    const int t = t0 + wave;
#pragma unroll
    for (int i = 0; i < 5; ++i) xr[i] = *(const unsigned*)(q + (size_t)t * D_ + 2 * lane + 128 * i);
#pragma unroll
    for (int i = 0; i < 5; ++i) wr[i] = *(const unsigned*)(w_r + 2 * lane + 128 * i);
  }

  if (wave == 12) {
    const gu64* f = (const gu64*)(ws + WS_F1 + (b % R1) * REP_STRIDE) + lane;
    for (int it = 0; it < SPIN_CAP; ++it) {
      bool ok = true;
#pragma unroll
      for (int i = 0; i < 4; ++i) ok &= __hip_atomic_load(f + 64 * i, RLX_AGENT) == tag;
      if (__all(ok)) break;
      __builtin_amdgcn_s_sleep(1);
    }
  }
  __syncthreads();
  STAMP(3);
  u32x4 uu = {0u, 0u, 0u, 0u};
  if (conv_on) {
    Draw = ssm_D[ch];
    uu = __builtin_amdgcn_raw_buffer_load_b128(rs, WS_KVT + (ch * S_ + 8 * tt) * 2, 0, AUX_SC1);
    *(u32x4*)(us + ((tt >> 1) & 1) * U_PLANE + ((tt >> 2) + 16) * 32 + 16 * (tt & 1)) = uu;
  }
  __syncthreads();
  STAMP(4);
  if (b < 216) {
    if (wave >= 12) {
      const int f0 = ((b & 7) * 27 + (b >> 3)) * 8;
#pragma unroll
      for (int i = 0; i < 5; ++i) {
        const int p = tt + 256 * i;
        const u16* src = ((p & 1) ? w_up : w_gate) + (size_t)(p >> 1) * F_ + f0;
        glds16(src, lbase + L_W4 + 16 * ((wave - 12) * 64 + 256 * i));
      }
    }
  }
  if (team < 2) conv_tiles<15>(kc, us, kc, wave & 3, lane);
  else if (team == 2) { if (odd) conv_tiles<6>(kc, us, kc, wave & 3, lane); else conv_tiles<9>(kc, us, kc, wave & 3, lane); }
  else __syncthreads();
  STAMP(5);
  __syncthreads();
  if (ywr) {
    const char* red = kc + (tt >> 2) * 144 + (tt & 3) * 32;
    f32x4 s0 = *(const f32x4*)(red), s1 = *(const f32x4*)(red + 16);
#pragma unroll
    for (int w = 1; w < 4; ++w) {
      s0 += *(const f32x4*)(red + w * 9216);
      s1 += *(const f32x4*)(red + w * 9216 + 16);
    }
    const float Dc = bf2f(Draw);
    const u32x4 st = {nosent(pk(rb2((f32x2){s0[0], s0[1]}) + rb2(unpk(uu[0]) * Dc))), nosent(pk(rb2((f32x2){s0[2], s0[3]}) + rb2(unpk(uu[1]) * Dc))),
                      nosent(pk(rb2((f32x2){s1[0], s1[1]}) + rb2(unpk(uu[2]) * Dc))), nosent(pk(rb2((f32x2){s1[2], s1[3]}) + rb2(unpk(uu[3]) * Dc)))};
    __builtin_amdgcn_raw_buffer_store_b128(st, rs, WS_Y2T + (ch * S_ + 8 * tt) * 2, 0, AUX_SC1);
  }
  __syncthreads();
  STAMP(6);
  if (b >= 216 && wave >= 13) {
    const int j5 = b - 216;
    const int c05 = 16 * ((j5 & 7) * 5 + (j5 >> 3));
#pragma unroll
    for (int i = 0; i < 6; ++i) {
      const int pb = ((0 * 6 + i) * 3 + (wave - 13)) * 64;
      const int p = pb + lane;
      glds16(w_down + (size_t)(p >> 1) * D_ + c05 + 8 * (p & 1), lbase + L_W5 + 16 * pb);
    }
  }

  f32x2 xf[5], ql[5], wf[5];
  if (wave < 8) {
#pragma unroll
    for (int i = 0; i < 5; ++i) { xf[i] = unpk(xr[i]); wf[i] = unpk(wr[i]); }
    const f32x2 s2 = ((xf[0] + xf[1]) + (xf[2] + xf[3])) + xf[4];
    const float mean1 = wave_sum(s2[0] + s2[1]) * (1.0f / D_);
    f32x2 q2 = {0.f, 0.f};
#pragma unroll
    for (int i = 0; i < 5; ++i) { const f32x2 d = xf[i] - mean1; q2 += d * d; }
    const float var1 = wave_sum(q2[0] + q2[1]) * (1.0f / D_);
    const float m1 = rb(mean1);
    const float sd1 = rb(sqrtf(rb(rb(var1) + 1e-5f)));
    const float rc1 = __builtin_amdgcn_rcpf(sd1);
#pragma unroll
    for (int i = 0; i < 5; ++i) ql[i] = rb2(fdiv2(rb2(xf[i] - m1), sd1, rc1));
    asm volatile("" : "+v"(ql[0]), "+v"(ql[1]), "+v"(ql[2]), "+v"(ql[3]), "+v"(ql[4]), "+v"(wf[0]), "+v"(wf[1]),
                 "+v"(wf[2]), "+v"(wf[3]), "+v"(wf[4]));
  }

  STAMP(7);
  {
    u16* ys = (u16*)(lds + L_TB);
    if (wave < 10) {
      u32x4 a = ones;
      __builtin_amdgcn_s_sleep(YSLEEP);
      for (int it = 0; it < SPIN_CAP; ++it) {
        a = __builtin_amdgcn_raw_buffer_load_b128(rs, WS_Y2T + (tid * S_ + t0) * 2, 0, AUX_SC1);
        if (__all(ready4(a))) break;
        __builtin_amdgcn_s_sleep(YPER);
      }
#pragma unroll
      for (int j = 0; j < 8; ++j) ys[j * 648 + tid] = (u16)((j & 1) ? (a[j >> 1] >> 16) : (a[j >> 1] & 0xffffu));
    }
    __syncthreads();
    STAMP(8);
  if (b >= 216 && wave >= 13) {
    const int j5 = b - 216;
    const int c05 = 16 * ((j5 & 7) * 5 + (j5 >> 3));
#pragma unroll
    for (int i = 0; i < 6; ++i) {
      const int pb = ((1 * 6 + i) * 3 + (wave - 13)) * 64;
      const int p = pb + lane;
      glds16(w_down + (size_t)(p >> 1) * D_ + c05 + 8 * (p & 1), lbase + L_W5 + 16 * pb);
    }
  }
    f32x2 hf[5], hn[5];
    float m2 = 0.f, sd2 = 1.f;
    if (wave < 8) {
#pragma unroll
      for (int i = 0; i < 5; ++i) {
        const unsigned yy = *(const unsigned*)&ys[wave * 648 + 2 * lane + 128 * i];
        hf[i] = rb2(xf[i] + rb2(ql[i] * unpk(yy)));
      }
      const f32x2 s2 = ((hf[0] + hf[1]) + (hf[2] + hf[3])) + hf[4];
      const float mean2 = wave_sum(s2[0] + s2[1]) * (1.0f / D_);
      f32x2 q2 = {0.f, 0.f};
#pragma unroll
      for (int i = 0; i < 5; ++i) { const f32x2 d = hf[i] - mean2; q2 += d * d; }
      const float var2 = wave_sum(q2[0] + q2[1]) * (1.0f / D_);
      m2 = rb(mean2);
      sd2 = rb(sqrtf(rb(rb(var2) + 1e-5f)));
      const float rc2 = __builtin_amdgcn_rcpf(sd2);
      f32x2 d2 = {0.f, 0.f};
#pragma unroll
      for (int i = 0; i < 5; ++i) {
        hn[i] = rb2(fdiv2(rb2(hf[i] - m2), sd2, rc2));
        d2 += hn[i] * wf[i];
      }
      const float dot = d2[0] + d2[1];
      const float rr = wave_sum(dot);
      unsigned rbits = f2bf(rr);
      if (rbits == 0xffffu) rbits = 0xfffeu;
      if (lane == 0) ((u16*)(lds + L_CAND))[wave] = (u16)rbits;
    }
    __syncthreads();
  if (b >= 216 && wave >= 13) {
    const int j5 = b - 216;
    const int c05 = 16 * ((j5 & 7) * 5 + (j5 >> 3));
#pragma unroll
    for (int i = 0; i < 6; ++i) {
      const int pb = ((2 * 6 + i) * 3 + (wave - 13)) * 64;
      const int p = pb + lane;
      glds16(w_down + (size_t)(p >> 1) * D_ + c05 + 8 * (p & 1), lbase + L_W5 + 16 * pb);
    }
  }
    if (wave == 8 && lane < RK) {
      const u32x4 kq = *(const u32x4*)(lds + L_CAND);
      __builtin_amdgcn_raw_buffer_store_b128(kq, rs, WS_KEYS + lane * REP_STRIDE + t0 * 2, 0, AUX_SC1);
    }
    if (wave < 8) {
      const int t = t0 + wave;
      unsigned* stg = (unsigned*)(lds + wave * 2560);
#pragma unroll
      for (int i = 0; i < 5; ++i) {
        stg[lane + 64 * i] = pk(hf[i] + hn[i]);
        stg[320 + lane + 64 * i] = pk(hf[i]);
      }
      __builtin_amdgcn_fence(__ATOMIC_RELEASE, "wavefront");
      __builtin_amdgcn_wave_barrier();
      __builtin_amdgcn_fence(__ATOMIC_ACQUIRE, "wavefront");
      const u32x4 h0 = *(const u32x4*)(stg + 320 + 4 * lane);
      __builtin_amdgcn_raw_buffer_store_b128(h0, rs, WS_H + (t * D_ + 8 * lane) * 2, 0, AUX_SC1);
      if (lane < 16) {
        const u32x4 h1 = *(const u32x4*)(stg + 320 + 256 + 4 * lane);
        __builtin_amdgcn_raw_buffer_store_b128(h1, rs, WS_H + (t * D_ + 512 + 8 * lane) * 2, 0, AUX_SC1);
      }
      if (lane == 0) {
        const u32x2 sv = {__float_as_uint(m2), __float_as_uint(sd2)};
        __builtin_amdgcn_raw_buffer_store_b64(sv, rs, WS_STATS + t * 8, 0, AUX_SC1);
      }
    }
    drain();
    if (wave < 8) {
      unsigned old = 0;
      if (lane == 0) old = atomicAdd((unsigned*)(lds + L_CAND + 400), 1u);
      old = __builtin_amdgcn_readfirstlane(old);
      if (old == 7u && lane < R2) __hip_atomic_store((gu64*)(ws + WS_F3 + lane * REP_STRIDE) + b, tag, RLX_AGENT);
    }
  }
  STAMP(9);

  unsigned* cand = (unsigned*)(lds + L_CAND);
  unsigned kk[8];
  const bool tkw = (wave >= 8 && wave < 12);
  const int tkt = tid - 512;
  if (tkw) {
    u32x4 gq = ones;
    __builtin_amdgcn_s_sleep(24);
    for (int it = 0; it < SPIN_CAP; ++it) {
      gq = __builtin_amdgcn_raw_buffer_load_b128(rs, WS_KEYS + (b % RK) * REP_STRIDE + tkt * 16, 0, AUX_SC1);
      if (__all(ready4(gq))) break;
      __builtin_amdgcn_s_sleep(10);
    }
#pragma unroll
    for (int j = 0; j < 8; ++j) {
      unsigned x = (j & 1) ? (gq[j >> 1] >> 16) : (gq[j >> 1] & 0xffffu);
      x = (x & 0x8000u) ? (~x & 0xffffu) : (x | 0x8000u);
      kk[j] = (x << 16) | (unsigned)(0xffff - 8 * tkt - j);
    }
    unsigned m = kk[0];
#pragma unroll
    for (int j = 1; j < 8; ++j) m = umax(m, kk[j]);
    const unsigned M1 = wave_umax(m);
    m = 0;
#pragma unroll
    for (int j = 0; j < 8; ++j) m = umax(m, kk[j] == M1 ? 0u : kk[j]);
    const unsigned M2 = wave_umax(m);
    if (lane == 0) { cand[64 + wave - 8] = M2; if (wave == 8) cand[68] = 0; }
  }
  __syncthreads();
  STAMP(10);
  if (tkw) {
    const unsigned th = min(min(cand[64], cand[65]), min(cand[66], cand[67]));
#pragma unroll
    for (int j = 0; j < 8; ++j) {
      if (kk[j] >= th) {
        const unsigned pos = atomicAdd(&cand[68], 1u);
        if (pos < 64) cand[pos] = kk[j];
      }
    }
  }
  __syncthreads();
  if (wave == 12) {
    const gu64* f = (const gu64*)(ws + WS_F3 + (b % R2) * REP_STRIDE) + lane;
    for (int it = 0; it < SPIN_CAP; ++it) {
      bool ok = true;
#pragma unroll
      for (int i = 0; i < 4; ++i) ok &= __hip_atomic_load(f + 64 * i, RLX_AGENT) == tag;
      if (__all(ok)) break;
      __builtin_amdgcn_s_sleep(1);
    }
  }
  const unsigned ncand = cand[68];
  if (ncand <= 64) {
    if (wave == 0) {
      const unsigned mine = (lane < (int)ncand) ? cand[lane] : 0u;
      unsigned rank = 0;
      for (unsigned i = 0; i < ncand; ++i) rank += (cand[i] > mine) ? 1u : 0u;
      if (lane < (int)ncand && rank < 8) cand[72 + rank] = mine;
    }
  } else {
    for (int rnd = 0; rnd < 8; ++rnd) {
      __syncthreads();
      if (tkw) {
        unsigned m = kk[0];
#pragma unroll
        for (int j = 1; j < 8; ++j) m = umax(m, kk[j]);
        const unsigned M = wave_umax(m);
        if (lane == 0) cand[64 + wave - 8] = M;
      }
      __syncthreads();
      const unsigned M = umax(umax(cand[64], cand[65]), umax(cand[66], cand[67]));
      if (tkw) {
#pragma unroll
        for (int j = 0; j < 8; ++j) kk[j] = (kk[j] == M) ? 0u : kk[j];
      }
      if (tid == 0) cand[72 + rnd] = M;
    }
  }
  __syncthreads();
  STAMP(11);
  if (wave >= 10) {
    for (int rr = wave - 10; rr < 8; rr += 6) {
      const int t = t0 + rr;
      bool routed = false;
#pragma unroll
      for (int j = 0; j < 8; ++j) routed |= (0xffff - (int)(cand[72 + j] & 0xffffu)) == t;
      if (!routed) {
        const unsigned* stg = (const unsigned*)(lds + rr * 2560);
        __builtin_amdgcn_raw_buffer_store_b128(*(const u32x4*)(stg + 4 * lane), ro, (t * D_ + 8 * lane) * 2, 0, AUX_SC1);
        if (lane < 16) __builtin_amdgcn_raw_buffer_store_b128(*(const u32x4*)(stg + 256 + 4 * lane), ro, (t * D_ + 512 + 8 * lane) * 2, 0, AUX_SC1);
      }
    }
  }
  const int tok = lane & 15, g = lane >> 4, i16 = lane & 15;
  float* red = (float*)(lds + L_RED);
  if (b < 216) {
    const int slab = (b & 7) * 27 + (b >> 3);
    const int f0 = slab * 8;
    if (wave < 10) {
      const unsigned mykey = cand[72 + (tok & 7)];
      const int myidx = 0xffff - (int)(mykey & 0xffffu);
      const u32x2 stv = __builtin_amdgcn_raw_buffer_load_b64(rs, WS_STATS + myidx * 8, 0, AUX_SC1);
      u32x4 hraw[2];
#pragma unroll
      for (int i = 0; i < 2; ++i)
        hraw[i] = __builtin_amdgcn_raw_buffer_load_b128(rs, WS_H + (myidx * D_ + 32 * (2 * wave + i) + 8 * g) * 2, 0, AUX_SC1);
      const float sm = __uint_as_float(stv[0]), ss = __uint_as_float(stv[1]);
      const float rcs = __builtin_amdgcn_rcpf(ss);
      f32x4 acc = {0.f, 0.f, 0.f, 0.f};
#pragma unroll
      for (int i = 0; i < 2; ++i) {
        const int k0 = 32 * (2 * wave + i);
        u32x4 HN;
#pragma unroll
        for (int d = 0; d < 4; ++d) {
          HN[d] = pk(fdiv2(rb2(unpk(hraw[i][d]) - sm), ss, rcs));
        }
        const char* p = lds + L_W4 + (k0 + 8 * g + (i16 >> 2)) * 32 + 8 * (i16 & 3);
        const s16x4 blo = tr_read(p);
        const s16x4 bhi = tr_read(p + 4 * 32);
        const bf16x8 bfrag = __builtin_shufflevector(blo, bhi, 0, 1, 2, 3, 4, 5, 6, 7);
        acc = __builtin_amdgcn_mfma_f32_16x16x32_bf16(__builtin_bit_cast(bf16x8, HN), bfrag, acc, 0, 0, 0);
      }
      if (g < 2) {
#pragma unroll
        for (int j = 0; j < 4; ++j) red[(wave * 8 + 4 * g + j) * 16 + i16] = acc[j];
      }
    }
    __syncthreads();
    STAMP(12);
    if (wave == 0) {
      const int tk = lane >> 3, col = lane & 7;
      float G = 0.f, U = 0.f;
#pragma unroll
      for (int w = 0; w < 10; ++w) {
        G += red[(w * 8 + tk) * 16 + col];
        U += red[(w * 8 + tk) * 16 + col + 8];
      }
      G = rb(G); U = rb(U);
      const float gg = rb(G / (1.0f + __expf(-G)));
      u16* stg = (u16*)(lds + L_CAND);
      unsigned gub = f2bf(gg * U);
      if (gub == 0xffffu) gub = 0xfffeu;
      stg[lane] = (u16)gub;
      __builtin_amdgcn_fence(__ATOMIC_RELEASE, "wavefront");
      __builtin_amdgcn_wave_barrier();
      __builtin_amdgcn_fence(__ATOMIC_ACQUIRE, "wavefront");
      {
        const u32x4 o = *(const u32x4*)(stg + 8 * (lane & 7));
        __builtin_amdgcn_raw_buffer_store_b128(o, rs, WS_GU + (lane >> 3) * GU_STRIDE + slab * 128 + (lane & 7) * 16, 0, AUX_SC1);
      }
      STAMP(13);
    }
  } else {
    const int j = b - 216;
    const int c0 = 16 * ((j & 7) * 5 + (j >> 3));
    unsigned key = 0, hv = 0;
    u32x2 stv = {0u, 0u};
    if (wave < 2) {
      const int tk = tid >> 4;
      key = cand[72 + tk];
      const int idx = 0xffff - (int)(key & 0xffffu);
      hv = (unsigned)(u16)__builtin_amdgcn_raw_buffer_load_b16(rs, WS_H + (idx * D_ + c0 + (tid & 15)) * 2, 0, AUX_SC1);
      stv = __builtin_amdgcn_raw_buffer_load_b64(rs, WS_STATS + idx * 8, 0, AUX_SC1);
    }
    {
      u32x4 af[4];
      __builtin_amdgcn_s_sleep(40);
      for (int it = 0; it < SPIN_CAP; ++it) {
        bool ok = true;
#pragma unroll
        for (int i = 0; i < 4; ++i) {
          const int ks = wave + 16 * i;
          const int ksc = ks < 54 ? ks : 0;
          af[i] = (u32x4){0u, 0u, 0u, 0u};
          if (tok < 8) af[i] = __builtin_amdgcn_raw_buffer_load_b128(rs, WS_GU + (j % R4) * GU_STRIDE + (4 * ksc + g) * 128 + tok * 16, 0, AUX_SC1);
        }
#pragma unroll
        for (int i = 0; i < 4; ++i) ok &= ready4(af[i]);
        if (__all(ok)) break;
        __builtin_amdgcn_s_sleep(2);
      }
      STAMP(12);
      f32x4 acc = {0.f, 0.f, 0.f, 0.f};
#pragma unroll
      for (int i = 0; i < 4; ++i) {
        const int ks = wave + 16 * i;
        if (ks < 54) {
          const int k0 = 32 * ks;
          const char* p = lds + L_W5 + (k0 + 8 * g + (i16 >> 2)) * 32 + 8 * (i16 & 3);
          const s16x4 blo = tr_read(p);
          const s16x4 bhi = tr_read(p + 4 * 32);
          const bf16x8 bfrag = __builtin_shufflevector(blo, bhi, 0, 1, 2, 3, 4, 5, 6, 7);
          acc = __builtin_amdgcn_mfma_f32_16x16x32_bf16(__builtin_bit_cast(bf16x8, af[i]), bfrag, acc, 0, 0, 0);
        }
      }
      if (g < 2) {
#pragma unroll
        for (int jj = 0; jj < 4; ++jj) red[(wave * 8 + 4 * g + jj) * 16 + i16] = acc[jj];
      }
    }
    __syncthreads();
    if (wave < 2) {
      const int tk = tid >> 4, col = tid & 15;
      float Y = 0.f;
#pragma unroll
      for (int w = 0; w < 16; ++w) Y += red[(w * 8 + tk) * 16 + col];
      Y = rb(Y);
      const unsigned ord = key >> 16;
      const unsigned rbits = (ord & 0x8000u) ? (ord & 0x7fffu) : (~ord & 0xffffu);
      const float rv = bf2f(rbits);
      const float rsg = rb(1.0f / (1.0f + __expf(-rv)));
      const float y = rb(Y * rsg);
      const float hval = bf2f(hv);
      const float hn = rb(rb(hval - __uint_as_float(stv[0])) / __uint_as_float(stv[1]));
      const float bo = rb(hn + y);
      const int idx = 0xffff - (int)(key & 0xffffu);
      __builtin_amdgcn_raw_buffer_store_b16((short)f2bf(hval + bo), ro, (idx * D_ + c0 + col) * 2, 0, AUX_SC1);
    }
    STAMP(13);
    if (b == 216 && tid == 0) __hip_atomic_store((gu64*)(ws + WS_E), tag, RLX_AGENT);
  }
}

extern "C" void kernel_launch(void* const* d_in, const int* in_sizes, int n_in, void* d_out, int out_size,
                              void* d_ws, size_t ws_size, hipStream_t stream) {
  fused_kernel<<<256, 1024, 0, stream>>>(
      (const u16*)d_in[0], (const u16*)d_in[1], (const u16*)d_in[2], (const u16*)d_in[3], (const u16*)d_in[4],
      (const u16*)d_in[5], (const u16*)d_in[7], (const u16*)d_in[8], (const u16*)d_in[9], (const u16*)d_in[10],
      (u16*)d_out, (char*)d_ws);
}
```

```cpp
#include <hip/hip_runtime.h>
#include <hip/hip_bf16.h>

typedef unsigned short u16;
typedef unsigned long long u64;
using bf16x8 = __attribute__((ext_vector_type(8))) short;
using s16x4 = __attribute__((ext_vector_type(4))) short;
using f32x16 = __attribute__((ext_vector_type(16))) float;
using f32x4 = __attribute__((ext_vector_type(4))) float;
using u32x4 = __attribute__((ext_vector_type(4))) unsigned;
using u32x2 = __attribute__((ext_vector_type(2))) unsigned;
typedef __attribute__((address_space(1))) u64 gu64;

#define DI __device__ __forceinline__
#define RLX_AGENT __ATOMIC_RELAXED, __HIP_MEMORY_SCOPE_AGENT
#define AUX_SC1 16

static constexpr int S_ = 2048;
static constexpr int D_ = 640;
static constexpr int F_ = 1728;

static constexpr unsigned MAT = S_ * D_ * 2;
static constexpr unsigned WS_KVT = 0;
static constexpr unsigned WS_Y2T = MAT;
static constexpr unsigned WS_H = 2 * MAT;
static constexpr unsigned WS_CTL = 3 * MAT;
static constexpr unsigned WS_E = WS_CTL;
static constexpr unsigned WS_STATS = WS_CTL + 4096;
static constexpr unsigned REP_STRIDE = 4352;
static constexpr int R1 = 4;
static constexpr int R2 = 16;
static constexpr int RK = 16;
static constexpr int R4 = 8;
static constexpr unsigned GU_STRIDE = 28160;
static constexpr unsigned WS_F1 = WS_CTL + 32768;
static constexpr unsigned WS_F2 = WS_F1 + R1 * REP_STRIDE;
static constexpr unsigned WS_F3 = WS_F2 + R2 * REP_STRIDE;
static constexpr unsigned WS_F4 = WS_F3 + R2 * REP_STRIDE;
static constexpr unsigned WS_KEYS = WS_F4 + R4 * REP_STRIDE;
static constexpr unsigned WS_GU = WS_KEYS + RK * REP_STRIDE;
static constexpr unsigned WS_BYTES = WS_GU + R4 * GU_STRIDE;

static constexpr int KC_STRIDE = 4160;
static constexpr int KC_BYTES = 8 * KC_STRIDE;
static constexpr int U_PLANE = 2560;
static constexpr int TEAM = 40960;
static constexpr int L_W4 = 122880;
static constexpr int L_W5 = 81920;
static constexpr int L_TB = 143360;
static constexpr int L_CAND = 154880;
static constexpr int L_RED = 155392;
static constexpr int LDS_BYTES = 163840;

DI float bf2f(unsigned x) { return __uint_as_float(x << 16); }
DI float lo2f(unsigned x) { return __uint_as_float(x << 16); }
DI float hi2f(unsigned x) { return __uint_as_float(x & 0xffff0000u); }
DI unsigned f2bf(float x) {
  __bf16 b = (__bf16)x;
  return (unsigned)__builtin_bit_cast(unsigned short, b);
}
DI float rb(float x) { return (float)((__bf16)x); }
DI unsigned pack2(float lo, float hi) { return f2bf(lo) | (f2bf(hi) << 16); }
typedef float f32x2 __attribute__((ext_vector_type(2)));
typedef __bf16 bf16x2_t __attribute__((ext_vector_type(2)));
DI f32x2 unpk(unsigned x) { return (f32x2){lo2f(x), hi2f(x)}; }
DI unsigned pk(f32x2 v) { return __builtin_bit_cast(unsigned, __builtin_convertvector(v, bf16x2_t)); }
DI f32x2 rb2(f32x2 v) { return unpk(pk(v)); }
DI unsigned nosent(unsigned p) {
  if ((p & 0xffffu) == 0xffffu) p ^= 1u;
  if ((p >> 16) == 0xffffu) p ^= 0x10000u;
  return p;
}
DI bool ready4(const u32x4 a) {
  bool ok = true;
#pragma unroll
  for (int d = 0; d < 4; ++d) ok &= ((a[d] & 0xffffu) != 0xffffu) && ((a[d] >> 16) != 0xffffu);
  return ok;
}
DI float fdiv(float a, float b, float rcpb) {
  const float q0 = a * rcpb;
  const float e = __builtin_fmaf(-q0, b, a);
  return __builtin_fmaf(e, rcpb, q0);
}

DI f32x2 fdiv2(f32x2 a, float b, float rcpb) {
  const f32x2 q0 = a * rcpb;
  const f32x2 e = __builtin_elementwise_fma(-q0, (f32x2){b, b}, a);
  return __builtin_elementwise_fma(e, (f32x2){rcpb, rcpb}, q0);
}

template <int CTRL>
DI int dpp_i(int v) { return __builtin_amdgcn_update_dpp(0, v, CTRL, 0xF, 0xF, true); }
DI float wave_sum(float v) {
  v += __int_as_float(dpp_i<0xB1>(__float_as_int(v)));
  v += __int_as_float(dpp_i<0x4E>(__float_as_int(v)));
  v += __int_as_float(dpp_i<0x141>(__float_as_int(v)));
  v += __int_as_float(dpp_i<0x140>(__float_as_int(v)));
  float a = __int_as_float(__builtin_amdgcn_readlane(__float_as_int(v), 0));
  float b = __int_as_float(__builtin_amdgcn_readlane(__float_as_int(v), 16));
  float c = __int_as_float(__builtin_amdgcn_readlane(__float_as_int(v), 32));
  float d = __int_as_float(__builtin_amdgcn_readlane(__float_as_int(v), 48));
  return (a + b) + (c + d);
}
DI unsigned umax(unsigned a, unsigned b) { return a > b ? a : b; }
DI unsigned wave_umax(unsigned v) {
  v = umax(v, (unsigned)dpp_i<0xB1>((int)v));
  v = umax(v, (unsigned)dpp_i<0x4E>((int)v));
  v = umax(v, (unsigned)dpp_i<0x141>((int)v));
  v = umax(v, (unsigned)dpp_i<0x140>((int)v));
  unsigned a = (unsigned)__builtin_amdgcn_readlane((int)v, 0);
  unsigned b = (unsigned)__builtin_amdgcn_readlane((int)v, 16);
  unsigned c = (unsigned)__builtin_amdgcn_readlane((int)v, 32);
  unsigned d = (unsigned)__builtin_amdgcn_readlane((int)v, 48);
  return umax(umax(a, b), umax(c, d));
}
typedef __attribute__((ext_vector_type(4))) short v4i16_t;
DI s16x4 tr_read(const char* p) {
  return __builtin_bit_cast(s16x4, __builtin_amdgcn_ds_read_tr16_b64_v4i16(
      (__attribute__((address_space(3))) v4i16_t*)p));
}
DI void glds16(const void* gsrc, unsigned lds_dst) {
  unsigned keep;
  asm volatile("s_mov_b32 %0, m0\n\ts_mov_b32 m0, %2\n\ts_nop 0\n\tglobal_load_lds_dwordx4 %1, off\n\ts_mov_b32 m0, %0"
               : "=&s"(keep) : "v"(gsrc), "s"(lds_dst) : "memory");
}
DI void drain() { asm volatile("s_waitcnt vmcnt(0)" ::: "memory"); }
static constexpr int SPIN_CAP = 1 << 20;
#define STAMP(i) do {} while (0)

template <int MASK>
DI void conv_tiles(const char* kc, const char* us, char* red, int w, int lane) {
  const int rp = lane & 15, g = lane >> 4;
  const char* pa = kc + (rp & 7) * KC_STRIDE + 2 * (2040 - 8 * (rp >> 3) + 8 * g) - 64 * w;
  const char* pb = us + (g >> 1) * U_PLANE + (rp + 16 - w) * 32 + 16 * (g & 1);
  f32x4 acc[4][2];
#pragma unroll
  for (int a = 0; a < 4; ++a) { acc[a][0] = (f32x4){0.f, 0.f, 0.f, 0.f}; acc[a][1] = (f32x4){0.f, 0.f, 0.f, 0.f}; }
#pragma unroll
  for (int mi = 0; mi < 16; ++mi) {
    constexpr int dummy = 0; (void)dummy;
    const int amin = mi >> 2;
    if ((MASK >> amin) == 0) continue;
    const bf16x8 a0 = *(const bf16x8*)(pa - 256 * mi);
    const bf16x8 a1 = *(const bf16x8*)(pa - 256 * mi - 32);
#pragma unroll
    for (int a = 0; a < 4; ++a) {
      if (a >= amin && ((MASK >> a) & 1)) {
        const bf16x8 bb = *(const bf16x8*)(pb + 512 * a - 128 * mi);
        acc[a][0] = __builtin_amdgcn_mfma_f32_16x16x32_bf16(a0, bb, acc[a][0], 0, 0, 0);
        acc[a][1] = __builtin_amdgcn_mfma_f32_16x16x32_bf16(a1, bb, acc[a][1], 0, 0, 0);
      }
    }
  }
  __syncthreads();
  char* rd = red + w * 9216 + rp * 144 + 16 * g;
#pragma unroll
  for (int a = 0; a < 4; ++a) {
    if ((MASK >> a) & 1) {
      *(f32x4*)(rd + a * 16 * 144) = acc[a][0];
      *(f32x4*)(rd + a * 16 * 144 + 64) = acc[a][1];
    }
  }
}

__global__ __launch_bounds__(1024) void fused_kernel(
    const u16* __restrict__ q, const u16* __restrict__ k, const u16* __restrict__ v,
    const u16* __restrict__ shift_k, const u16* __restrict__ ssm_k, const u16* __restrict__ ssm_D,
    const u16* __restrict__ w_r, const u16* __restrict__ w_gate, const u16* __restrict__ w_up,
    const u16* __restrict__ w_down, u16* out, char* ws) {
  __shared__ __attribute__((aligned(16))) char lds[LDS_BYTES];
  asm volatile("" ::"s"(q), "s"(k), "s"(v), "s"(shift_k), "s"(ssm_k), "s"(ssm_D), "s"(w_r), "s"(w_gate), "s"(w_up),
               "s"(w_down), "s"(out), "s"(ws));
  const int b = blockIdx.x;
  const int grp = b & 7, ib = b >> 3;
  const int tid = threadIdx.x, lane = tid & 63;
  const int wave = __builtin_amdgcn_readfirstlane(tid >> 6);
  const int team = wave >> 2, tt = tid & 255;
  const __amdgpu_buffer_rsrc_t rs = __builtin_amdgcn_make_buffer_rsrc(ws, 0, WS_BYTES, 0x00020000);
  const __amdgpu_buffer_rsrc_t ro = __builtin_amdgcn_make_buffer_rsrc(out, 0, MAT, 0x00020000);
  const u32x4 ones = {0xffffffffu, 0xffffffffu, 0xffffffffu, 0xffffffffu};
  STAMP(0);

  const int p1tok = tid / 10, p1chunk = tid - 10 * p1tok;
  const int p1c = 80 * grp + 8 * p1chunk;
  const bool conv_on = team < 3;
  const int chbase = 80 * grp + 5 * (ib >> 1);
  const int odd = ib & 1;
  const int ch = chbase + (team >= 2 ? 2 : (odd ? 3 + team : team));
  const int t0 = (grp * 32 + ib) * 8;
  const unsigned lbase = (unsigned)(size_t)lds;
  u32x4 kown = {0u, 0u, 0u, 0u}, vr = {0u, 0u, 0u, 0u};
  u64 eraw = 0;
  if (wave < 10) {
    const int t = 64 * ib + p1tok;
    kown = *(const u32x4*)(k + (size_t)t * D_ + p1c);
    vr = *(const u32x4*)(v + (size_t)t * D_ + p1c);
    if (wave == 0) eraw = __hip_atomic_load((gu64*)(ws + WS_E), RLX_AGENT);
    *(u32x4*)(lds + (p1tok + 3) * 176 + 16 * p1chunk) = kown;
    asm volatile("" : "+v"(vr), "+v"(eraw));
  } else if (wave == 10) {
    if (lane < 30) {
      const int hr = lane / 10, hc = lane - 10 * hr;
      const int t = 64 * ib - 3 + hr;
      u32x4 kh = {0u, 0u, 0u, 0u};
      if (t >= 0) kh = *(const u32x4*)(k + (size_t)t * D_ + 80 * grp + 8 * hc);
      *(u32x4*)(lds + hr * 176 + 16 * hc) = kh;
    }
    if (b < 216)
      __builtin_amdgcn_raw_buffer_store_b128(ones, rs, WS_GU + (lane >> 3) * GU_STRIDE + ((b & 7) * 27 + (b >> 3)) * 128 + (lane & 7) * 16, 0, AUX_SC1);
  } else if (wave == 11) {
    if (lane < 40) {
      const u32x4 d = *(const u32x4*)(shift_k + (size_t)grp * 320 + 8 * lane);
      *(f32x4*)(lds + 12288 + 32 * lane) = (f32x4){lo2f(d[0]), lo2f(d[2]), hi2f(d[0]), hi2f(d[2])};
      *(f32x4*)(lds + 12288 + 32 * lane + 16) = (f32x4){lo2f(d[1]), lo2f(d[3]), hi2f(d[1]), hi2f(d[3])};
    }
    if (lane < RK) __builtin_amdgcn_raw_buffer_store_b128(ones, rs, WS_KEYS + lane * REP_STRIDE + t0 * 2, 0, AUX_SC1);
  } else if (wave == 15) {
    const char* tp = ws + (size_t)(lane & 31) * (WS_BYTES / 32 & ~15u);
    if (lane >= 32) tp = (const char*)out + (size_t)(lane & 31) * (MAT / 32);
    glds16(tp, lbase + L_RED);
  } else {
    const int chk = chbase + (wave == 14 ? 2 : (odd ? 3 + (wave - 12) : (wave - 12)));
#pragma unroll
    for (int i = 0; i < 4; ++i)
      glds16(ssm_k + (size_t)chk * S_ + 512 * i + 8 * lane, lbase + L_W4 + 4096 * (wave - 12) + 1024 * i);
  }
  __syncthreads();
  unsigned Draw = 0;
  if (wave == 15) {
    const unsigned wbytes = D_ * F_ * 2;
#pragma unroll
    for (int i = 0; i < 9; ++i) {
      const unsigned o = (unsigned)b * 8640u + i * 1024u + lane * 16u;
      if (i < 8 || lane < 28) {
        glds16((const char*)w_gate + o, lbase + L_RED);
        glds16((const char*)w_up + o, lbase + L_RED);
        glds16((const char*)w_down + o, lbase + L_RED);
      }
    }
    (void)wbytes;
  }
  if (wave < 10) {
    u32x4 kr[3];
#pragma unroll
    for (int i = 0; i < 3; ++i) kr[i] = *(const u32x4*)(lds + (p1tok + i) * 176 + 16 * p1chunk);
#pragma unroll
    for (int jp = 0; jp < 4; ++jp) {
      const f32x4 wa = *(const f32x4*)(lds + 12288 + 128 * p1chunk + 32 * jp);
      const f32x4 wb = *(const f32x4*)(lds + 12288 + 128 * p1chunk + 32 * jp + 16);
      const f32x2 w0 = {wa[0], wa[1]}, w1 = {wa[2], wa[3]}, w2 = {wb[0], wb[1]}, w3 = {wb[2], wb[3]};
      f32x2 ks = rb2(unpk(kr[0][jp]) * w3);
      ks = rb2(ks + rb2(unpk(kr[1][jp]) * w2));
      ks = rb2(ks + rb2(unpk(kr[2][jp]) * w1));
      ks = rb2(ks + rb2(unpk(kown[jp]) * w0));
      const unsigned o = pk(ks * unpk(vr[jp]));
      char* dst = lds + L_TB + (8 * p1chunk + 2 * jp) * 128 + 16 * p1chunk + 2 * p1tok;
      *(u16*)dst = (u16)(o & 0xffffu);
      *(u16*)(dst + 128) = (u16)(o >> 16);
    }
  }
  asm volatile("" : "+v"(eraw));
  const u64 tag0 = eraw + 1ull;
  if (tid == 0) { *(u64*)(lds + L_CAND + 384) = tag0; *(unsigned*)(lds + L_CAND + 400) = 0u; }
  if (wave >= 12 && wave < 15) drain();
  __syncthreads();
  STAMP(1);
  if (tid < 640) {
    const int row = tid >> 3, piece = tid & 7;
    const u32x4 d = *(const u32x4*)(lds + L_TB + row * 128 + 16 * (row >> 3) + 16 * piece);
    __builtin_amdgcn_raw_buffer_store_b128(d, rs, WS_KVT + ((80 * grp + row) * S_ + 64 * ib + 8 * piece) * 2, 0, AUX_SC1);
  }
  const u64 tag = *(const u64*)(lds + L_CAND + 384);
  char* kc = lds + TEAM * team;
  char* us = kc + KC_BYTES;
  if (conv_on) {
    const u32x4 z4 = {0u, 0u, 0u, 0u};
    u32x4 klo = *(const u32x4*)(lds + L_W4 + 4096 * team + 16 * (tt < 255 ? 254 - tt : 0));
    const u32x4 khi = *(const u32x4*)(lds + L_W4 + 4096 * team + 16 * (255 - tt));
    if (tt >= 255) klo = z4;
    if (tt < 64) *(u32x4*)(us + (tt >> 5) * U_PLANE + 16 * (tt & 31)) = z4;
    if (tt < 32) *(u32x4*)(kc + (tt >> 2) * KC_STRIDE + 16 * (256 + (tt & 3))) = z4;
    const unsigned W[8] = {klo[0], klo[1], klo[2], klo[3], khi[0], khi[1], khi[2], khi[3]};
    unsigned E[7], O[7];
#pragma unroll
    for (int d = 0; d < 7; ++d) {
      E[d] = (W[7 - d] >> 16) | (W[7 - d] << 16);
      O[d] = (W[7 - d] & 0xffffu) | (W[6 - d] & 0xffff0000u);
    }
    char* dst = kc + 16 * tt;
#pragma unroll
    for (int qq = 0; qq < 4; ++qq) {
      *(u32x4*)(dst + (7 - 2 * qq) * KC_STRIDE) = (u32x4){E[qq], E[qq + 1], E[qq + 2], E[qq + 3]};
      *(u32x4*)(dst + (6 - 2 * qq) * KC_STRIDE) = (u32x4){O[qq], O[qq + 1], O[qq + 2], O[qq + 3]};
    }
  }
  if (wave < 12) drain();
  __syncthreads();
  if (tid < R1) __hip_atomic_store((gu64*)(ws + WS_F1 + tid * REP_STRIDE) + grp * 32 + ib, tag, RLX_AGENT);
  STAMP(2);

  unsigned xr[5], wr[5];
  if (wave < 8) {
    const int t = t0 + wave;
#pragma unroll
    for (int i = 0; i < 5; ++i) xr[i] = *(const unsigned*)(q + (size_t)t * D_ + 2 * lane + 128 * i);
#pragma unroll
    for (int i = 0; i < 5; ++i) wr[i] = *(const unsigned*)(w_r + 2 * lane + 128 * i);
  }

  if (wave == 12) {
    const gu64* f = (const gu64*)(ws + WS_F1 + (ib % R1) * REP_STRIDE) + grp * 32 + (lane & 31);
    for (int it = 0; it < SPIN_CAP; ++it) {
      const bool ok = __hip_atomic_load(f, RLX_AGENT) == tag;
      if (__all(ok)) break;
      __builtin_amdgcn_s_sleep(1);
    }
  }
  __syncthreads();
  STAMP(3);
  u32x4 uu = {0u, 0u, 0u, 0u};
  if (conv_on) {
    Draw = ssm_D[ch];
    uu = __builtin_amdgcn_raw_buffer_load_b128(rs, WS_KVT + (ch * S_ + 8 * tt) * 2, 0, AUX_SC1);
    *(u32x4*)(us + ((tt >> 1) & 1) * U_PLANE + ((tt >> 2) + 16) * 32 + 16 * (tt & 1)) = uu;
  }
  __syncthreads();
  STAMP(4);
  if (b < 216) {
    if (wave >= 12) {
      const int f0 = ((b & 7) * 27 + (b >> 3)) * 8;
#pragma unroll
      for (int i = 0; i < 5; ++i) {
        const int p = tt + 256 * i;
        const u16* src = ((p & 1) ? w_up : w_gate) + (size_t)(p >> 1) * F_ + f0;
        glds16(src, lbase + L_W4 + 16 * ((wave - 12) * 64 + 256 * i));
      }
    }
  } else if (wave >= 12) {
    const int j5 = b - 216;
    const int c05 = 16 * ((j5 & 7) * 5 + (j5 >> 3));
    for (int rb0 = (wave - 12) * 64; rb0 < F_; rb0 += 256)
      glds16(w_down + (size_t)(rb0 + lane) * D_ + c05, lbase + L_RED);
  }
  if (team < 2) conv_tiles<15>(kc, us, kc, wave & 3, lane);
  else if (team == 2) { if (odd) conv_tiles<6>(kc, us, kc, wave & 3, lane); else conv_tiles<9>(kc, us, kc, wave & 3, lane); }
  else __syncthreads();
  STAMP(5);
  __syncthreads();
  const int myal = tt >> 6;
  if (team < 2 || (team == 2 && ((myal == 1 || myal == 2) == (odd != 0)))) {
    const char* red = kc + (tt >> 2) * 144 + (tt & 3) * 32;
    f32x4 s0 = *(const f32x4*)(red), s1 = *(const f32x4*)(red + 16);
#pragma unroll
    for (int w = 1; w < 4; ++w) {
      s0 += *(const f32x4*)(red + w * 9216);
      s1 += *(const f32x4*)(red + w * 9216 + 16);
    }
    const float Dc = bf2f(Draw);
    const u32x4 st = {pk(rb2((f32x2){s0[0], s0[1]}) + rb2(unpk(uu[0]) * Dc)), pk(rb2((f32x2){s0[2], s0[3]}) + rb2(unpk(uu[1]) * Dc)),
                      pk(rb2((f32x2){s1[0], s1[1]}) + rb2(unpk(uu[2]) * Dc)), pk(rb2((f32x2){s1[2], s1[3]}) + rb2(unpk(uu[3]) * Dc))};
    __builtin_amdgcn_raw_buffer_store_b128(st, rs, WS_Y2T + (ch * S_ + 8 * tt) * 2, 0, AUX_SC1);
  }
  if (conv_on) drain();
  __syncthreads();
  if (tid < R2) __hip_atomic_store((gu64*)(ws + WS_F2 + tid * REP_STRIDE) + b, tag, RLX_AGENT);
  STAMP(6);
  if (b >= 216 && wave >= 13) {
    const int j5 = b - 216;
    const int c05 = 16 * ((j5 & 7) * 5 + (j5 >> 3));
    for (int pb = (wave - 13) * 64; pb < 3456; pb += 192) {
      const int p = pb + lane;
      glds16(w_down + (size_t)(p >> 1) * D_ + c05 + 8 * (p & 1), lbase + L_W5 + 16 * pb);
    }
  }

  f32x2 xf[5], ql[5], wf[5];
  if (wave < 8) {
#pragma unroll
    for (int i = 0; i < 5; ++i) { xf[i] = unpk(xr[i]); wf[i] = unpk(wr[i]); }
    const f32x2 s2 = ((xf[0] + xf[1]) + (xf[2] + xf[3])) + xf[4];
    const float mean1 = wave_sum(s2[0] + s2[1]) * (1.0f / D_);
    f32x2 q2 = {0.f, 0.f};
#pragma unroll
    for (int i = 0; i < 5; ++i) { const f32x2 d = xf[i] - mean1; q2 += d * d; }
    const float var1 = wave_sum(q2[0] + q2[1]) * (1.0f / D_);
    const float m1 = rb(mean1);
    const float sd1 = rb(sqrtf(rb(rb(var1) + 1e-5f)));
    const float rc1 = __builtin_amdgcn_rcpf(sd1);
#pragma unroll
    for (int i = 0; i < 5; ++i) ql[i] = rb2(fdiv2(rb2(xf[i] - m1), sd1, rc1));
    asm volatile("" : "+v"(ql[0]), "+v"(ql[1]), "+v"(ql[2]), "+v"(ql[3]), "+v"(ql[4]), "+v"(wf[0]), "+v"(wf[1]),
                 "+v"(wf[2]), "+v"(wf[3]), "+v"(wf[4]));
  }

  if (wave == 11) {
    const gu64* f = (const gu64*)(ws + WS_F2 + (b % R2) * REP_STRIDE) + lane;
    for (int it = 0; it < SPIN_CAP; ++it) {
      bool ok = true;
#pragma unroll
      for (int i = 0; i < 4; ++i) ok &= __hip_atomic_load(f + 64 * i, RLX_AGENT) == tag;
      if (__all(ok)) break;
      __builtin_amdgcn_s_sleep(1);
    }
  }
  __syncthreads();
  STAMP(7);
  {
    u16* ys = (u16*)(lds + L_TB);
    if (wave < 10) {
      const u32x4 a = __builtin_amdgcn_raw_buffer_load_b128(rs, WS_Y2T + (tid * S_ + t0) * 2, 0, AUX_SC1);
#pragma unroll
      for (int j = 0; j < 8; ++j) ys[j * 648 + tid] = (u16)((j & 1) ? (a[j >> 1] >> 16) : (a[j >> 1] & 0xffffu));
    }
    __syncthreads();
    STAMP(8);
    f32x2 hf[5], hn[5];
    float m2 = 0.f, sd2 = 1.f;
    if (wave < 8) {
#pragma unroll
      for (int i = 0; i < 5; ++i) {
        const unsigned yy = *(const unsigned*)&ys[wave * 648 + 2 * lane + 128 * i];
        hf[i] = rb2(xf[i] + rb2(ql[i] * unpk(yy)));
      }
      const f32x2 s2 = ((hf[0] + hf[1]) + (hf[2] + hf[3])) + hf[4];
      const float mean2 = wave_sum(s2[0] + s2[1]) * (1.0f / D_);
      f32x2 q2 = {0.f, 0.f};
#pragma unroll
      for (int i = 0; i < 5; ++i) { const f32x2 d = hf[i] - mean2; q2 += d * d; }
      const float var2 = wave_sum(q2[0] + q2[1]) * (1.0f / D_);
      m2 = rb(mean2);
      sd2 = rb(sqrtf(rb(rb(var2) + 1e-5f)));
      const float rc2 = __builtin_amdgcn_rcpf(sd2);
      f32x2 d2 = {0.f, 0.f};
#pragma unroll
      for (int i = 0; i < 5; ++i) {
        hn[i] = rb2(fdiv2(rb2(hf[i] - m2), sd2, rc2));
        d2 += hn[i] * wf[i];
      }
      const float dot = d2[0] + d2[1];
      const float rr = wave_sum(dot);
      unsigned rbits = f2bf(rr);
      if (rbits == 0xffffu) rbits = 0xfffeu;
      if (lane == 0) ((u16*)(lds + L_CAND))[wave] = (u16)rbits;
    }
    __syncthreads();
    if (wave == 8 && lane < RK) {
      const u32x4 kq = *(const u32x4*)(lds + L_CAND);
      __builtin_amdgcn_raw_buffer_store_b128(kq, rs, WS_KEYS + lane * REP_STRIDE + t0 * 2, 0, AUX_SC1);
    }
    if (wave < 8) {
      const int t = t0 + wave;
      unsigned* stg = (unsigned*)(lds + wave * 2560);
#pragma unroll
      for (int i = 0; i < 5; ++i) {
        stg[lane + 64 * i] = pk(hf[i] + hn[i]);
        stg[320 + lane + 64 * i] = pk(hf[i]);
      }
      __builtin_amdgcn_fence(__ATOMIC_RELEASE, "wavefront");
      __builtin_amdgcn_wave_barrier();
      __builtin_amdgcn_fence(__ATOMIC_ACQUIRE, "wavefront");
      const u32x4 h0 = *(const u32x4*)(stg + 320 + 4 * lane);
      __builtin_amdgcn_raw_buffer_store_b128(h0, rs, WS_H + (t * D_ + 8 * lane) * 2, 0, AUX_SC1);
      if (lane < 16) {
        const u32x4 h1 = *(const u32x4*)(stg + 320 + 256 + 4 * lane);
        __builtin_amdgcn_raw_buffer_store_b128(h1, rs, WS_H + (t * D_ + 512 + 8 * lane) * 2, 0, AUX_SC1);
      }
      if (lane == 0) {
        const u32x2 sv = {__float_as_uint(m2), __float_as_uint(sd2)};
        __builtin_amdgcn_raw_buffer_store_b64(sv, rs, WS_STATS + t * 8, 0, AUX_SC1);
      }
    }
    drain();
    if (wave < 8) {
      unsigned old = 0;
      if (lane == 0) old = atomicAdd((unsigned*)(lds + L_CAND + 400), 1u);
      old = __builtin_amdgcn_readfirstlane(old);
      if (old == 7u && lane < R2) __hip_atomic_store((gu64*)(ws + WS_F3 + lane * REP_STRIDE) + b, tag, RLX_AGENT);
    }
  }
  STAMP(9);

  unsigned* cand = (unsigned*)(lds + L_CAND);
  unsigned kk[8];
  const bool tkw = (wave >= 8 && wave < 12);
  const int tkt = tid - 512;
  if (tkw) {
    u32x4 gq = ones;
    __builtin_amdgcn_s_sleep(24);
    for (int it = 0; it < SPIN_CAP; ++it) {
      gq = __builtin_amdgcn_raw_buffer_load_b128(rs, WS_KEYS + (b % RK) * REP_STRIDE + tkt * 16, 0, AUX_SC1);
      if (__all(ready4(gq))) break;
      __builtin_amdgcn_s_sleep(10);
    }
#pragma unroll
    for (int j = 0; j < 8; ++j) {
      unsigned x = (j & 1) ? (gq[j >> 1] >> 16) : (gq[j >> 1] & 0xffffu);
      x = (x & 0x8000u) ? (~x & 0xffffu) : (x | 0x8000u);
      kk[j] = (x << 16) | (unsigned)(0xffff - 8 * tkt - j);
    }
    unsigned m = kk[0];
#pragma unroll
    for (int j = 1; j < 8; ++j) m = umax(m, kk[j]);
    const unsigned M1 = wave_umax(m);
    m = 0;
#pragma unroll
    for (int j = 0; j < 8; ++j) m = umax(m, kk[j] == M1 ? 0u : kk[j]);
    const unsigned M2 = wave_umax(m);
    if (lane == 0) { cand[64 + wave - 8] = M2; if (wave == 8) cand[68] = 0; }
  }
  __syncthreads();
  STAMP(10);
  if (tkw) {
    const unsigned th = min(min(cand[64], cand[65]), min(cand[66], cand[67]));
#pragma unroll
    for (int j = 0; j < 8; ++j) {
      if (kk[j] >= th) {
        const unsigned pos = atomicAdd(&cand[68], 1u);
        if (pos < 64) cand[pos] = kk[j];
      }
    }
  }
  __syncthreads();
  if (wave == 12) {
    const gu64* f = (const gu64*)(ws + WS_F3 + (b % R2) * REP_STRIDE) + lane;
    for (int it = 0; it < SPIN_CAP; ++it) {
      bool ok = true;
#pragma unroll
      for (int i = 0; i < 4; ++i) ok &= __hip_atomic_load(f + 64 * i, RLX_AGENT) == tag;
      if (__all(ok)) break;
      __builtin_amdgcn_s_sleep(1);
    }
  }
  const unsigned ncand = cand[68];
  if (ncand <= 64) {
    if (wave == 0) {
      const unsigned mine = (lane < (int)ncand) ? cand[lane] : 0u;
      unsigned rank = 0;
      for (unsigned i = 0; i < ncand; ++i) rank += (cand[i] > mine) ? 1u : 0u;
      if (lane < (int)ncand && rank < 8) cand[72 + rank] = mine;
    }
  } else {
    for (int rnd = 0; rnd < 8; ++rnd) {
      __syncthreads();
      if (tkw) {
        unsigned m = kk[0];
#pragma unroll
        for (int j = 1; j < 8; ++j) m = umax(m, kk[j]);
        const unsigned M = wave_umax(m);
        if (lane == 0) cand[64 + wave - 8] = M;
      }
      __syncthreads();
      const unsigned M = umax(umax(cand[64], cand[65]), umax(cand[66], cand[67]));
      if (tkw) {
#pragma unroll
        for (int j = 0; j < 8; ++j) kk[j] = (kk[j] == M) ? 0u : kk[j];
      }
      if (tid == 0) cand[72 + rnd] = M;
    }
  }
  __syncthreads();
  STAMP(11);
  if (wave >= 10) {
    for (int rr = wave - 10; rr < 8; rr += 6) {
      const int t = t0 + rr;
      bool routed = false;
#pragma unroll
      for (int j = 0; j < 8; ++j) routed |= (0xffff - (int)(cand[72 + j] & 0xffffu)) == t;
      if (!routed) {
        const unsigned* stg = (const unsigned*)(lds + rr * 2560);
        __builtin_amdgcn_raw_buffer_store_b128(*(const u32x4*)(stg + 4 * lane), ro, (t * D_ + 8 * lane) * 2, 0, AUX_SC1);
        if (lane < 16) __builtin_amdgcn_raw_buffer_store_b128(*(const u32x4*)(stg + 256 + 4 * lane), ro, (t * D_ + 512 + 8 * lane) * 2, 0, AUX_SC1);
      }
    }
  }
  const int tok = lane & 15, g = lane >> 4, i16 = lane & 15;
  float* red = (float*)(lds + L_RED);
  if (b < 216) {
    const int slab = (b & 7) * 27 + (b >> 3);
    const int f0 = slab * 8;
    if (wave < 10) {
      const unsigned mykey = cand[72 + (tok & 7)];
      const int myidx = 0xffff - (int)(mykey & 0xffffu);
      const u32x2 stv = __builtin_amdgcn_raw_buffer_load_b64(rs, WS_STATS + myidx * 8, 0, AUX_SC1);
      u32x4 hraw[2];
#pragma unroll
      for (int i = 0; i < 2; ++i)
        hraw[i] = __builtin_amdgcn_raw_buffer_load_b128(rs, WS_H + (myidx * D_ + 32 * (2 * wave + i) + 8 * g) * 2, 0, AUX_SC1);
      const float sm = __uint_as_float(stv[0]), ss = __uint_as_float(stv[1]);
      const float rcs = __builtin_amdgcn_rcpf(ss);
      f32x4 acc = {0.f, 0.f, 0.f, 0.f};
#pragma unroll
      for (int i = 0; i < 2; ++i) {
        const int k0 = 32 * (2 * wave + i);
        u32x4 HN;
#pragma unroll
        for (int d = 0; d < 4; ++d) {
          HN[d] = pk(fdiv2(rb2(unpk(hraw[i][d]) - sm), ss, rcs));
        }
        const char* p = lds + L_W4 + (k0 + 8 * g + (i16 >> 2)) * 32 + 8 * (i16 & 3);
        const s16x4 blo = tr_read(p);
        const s16x4 bhi = tr_read(p + 4 * 32);
        const bf16x8 bfrag = __builtin_shufflevector(blo, bhi, 0, 1, 2, 3, 4, 5, 6, 7);
        acc = __builtin_amdgcn_mfma_f32_16x16x32_bf16(__builtin_bit_cast(bf16x8, HN), bfrag, acc, 0, 0, 0);
      }
      if (g < 2) {
#pragma unroll
        for (int j = 0; j < 4; ++j) red[(wave * 8 + 4 * g + j) * 16 + i16] = acc[j];
      }
    }
    __syncthreads();
    STAMP(12);
    if (wave == 0) {
      const int tk = lane >> 3, col = lane & 7;
      float G = 0.f, U = 0.f;
#pragma unroll
      for (int w = 0; w < 10; ++w) {
        G += red[(w * 8 + tk) * 16 + col];
        U += red[(w * 8 + tk) * 16 + col + 8];
      }
      G = rb(G); U = rb(U);
      const float gg = rb(G / (1.0f + __expf(-G)));
      u16* stg = (u16*)(lds + L_CAND);
      unsigned gub = f2bf(gg * U);
      if (gub == 0xffffu) gub = 0xfffeu;
      stg[lane] = (u16)gub;
      __builtin_amdgcn_fence(__ATOMIC_RELEASE, "wavefront");
      __builtin_amdgcn_wave_barrier();
      __builtin_amdgcn_fence(__ATOMIC_ACQUIRE, "wavefront");
      {
        const u32x4 o = *(const u32x4*)(stg + 8 * (lane & 7));
        __builtin_amdgcn_raw_buffer_store_b128(o, rs, WS_GU + (lane >> 3) * GU_STRIDE + slab * 128 + (lane & 7) * 16, 0, AUX_SC1);
      }
      STAMP(13);
    }
  } else {
    const int j = b - 216;
    const int c0 = 16 * ((j & 7) * 5 + (j >> 3));
    unsigned key = 0, hv = 0;
    u32x2 stv = {0u, 0u};
    if (wave < 2) {
      const int tk = tid >> 4;
      key = cand[72 + tk];
      const int idx = 0xffff - (int)(key & 0xffffu);
      hv = (unsigned)(u16)__builtin_amdgcn_raw_buffer_load_b16(rs, WS_H + (idx * D_ + c0 + (tid & 15)) * 2, 0, AUX_SC1);
      stv = __builtin_amdgcn_raw_buffer_load_b64(rs, WS_STATS + idx * 8, 0, AUX_SC1);
    }
    {
      u32x4 af[4];
      __builtin_amdgcn_s_sleep(40);
      for (int it = 0; it < SPIN_CAP; ++it) {
        bool ok = true;
#pragma unroll
        for (int i = 0; i < 4; ++i) {
          const int ks = wave + 16 * i;
          const int ksc = ks < 54 ? ks : 0;
          af[i] = (u32x4){0u, 0u, 0u, 0u};
          if (tok < 8) af[i] = __builtin_amdgcn_raw_buffer_load_b128(rs, WS_GU + (j % R4) * GU_STRIDE + (4 * ksc + g) * 128 + tok * 16, 0, AUX_SC1);
        }
#pragma unroll
        for (int i = 0; i < 4; ++i) ok &= ready4(af[i]);
        if (__all(ok)) break;
        __builtin_amdgcn_s_sleep(2);
      }
      STAMP(12);
      f32x4 acc = {0.f, 0.f, 0.f, 0.f};
#pragma unroll
      for (int i = 0; i < 4; ++i) {
        const int ks = wave + 16 * i;
        if (ks < 54) {
          const int k0 = 32 * ks;
          const char* p = lds + L_W5 + (k0 + 8 * g + (i16 >> 2)) * 32 + 8 * (i16 & 3);
          const s16x4 blo = tr_read(p);
          const s16x4 bhi = tr_read(p + 4 * 32);
          const bf16x8 bfrag = __builtin_shufflevector(blo, bhi, 0, 1, 2, 3, 4, 5, 6, 7);
          acc = __builtin_amdgcn_mfma_f32_16x16x32_bf16(__builtin_bit_cast(bf16x8, af[i]), bfrag, acc, 0, 0, 0);
        }
      }
      if (g < 2) {
#pragma unroll
        for (int jj = 0; jj < 4; ++jj) red[(wave * 8 + 4 * g + jj) * 16 + i16] = acc[jj];
      }
    }
    __syncthreads();
    if (wave < 2) {
      const int tk = tid >> 4, col = tid & 15;
      float Y = 0.f;
#pragma unroll
      for (int w = 0; w < 16; ++w) Y += red[(w * 8 + tk) * 16 + col];
      Y = rb(Y);
      const unsigned ord = key >> 16;
      const unsigned rbits = (ord & 0x8000u) ? (ord & 0x7fffu) : (~ord & 0xffffu);
      const float rv = bf2f(rbits);
      const float rsg = rb(1.0f / (1.0f + __expf(-rv)));
      const float y = rb(Y * rsg);
      const float hval = bf2f(hv);
      const float hn = rb(rb(hval - __uint_as_float(stv[0])) / __uint_as_float(stv[1]));
      const float bo = rb(hn + y);
      const int idx = 0xffff - (int)(key & 0xffffu);
      __builtin_amdgcn_raw_buffer_store_b16((short)f2bf(hval + bo), ro, (idx * D_ + c0 + col) * 2, 0, AUX_SC1);
    }
    STAMP(13);
    if (b == 216 && tid == 0) __hip_atomic_store((gu64*)(ws + WS_E), tag, RLX_AGENT);
  }
}

extern "C" void kernel_launch(void* const* d_in, const int* in_sizes, int n_in, void* d_out, int out_size,
                              void* d_ws, size_t ws_size, hipStream_t stream) {
  fused_kernel<<<256, 1024, 0, stream>>>(
      (const u16*)d_in[0], (const u16*)d_in[1], (const u16*)d_in[2], (const u16*)d_in[3], (const u16*)d_in[4],
      (const u16*)d_in[5], (const u16*)d_in[7], (const u16*)d_in[8], (const u16*)d_in[9], (const u16*)d_in[10],
      (u16*)d_out, (char*)d_ws);
}
```

```cpp
#include <hip/hip_runtime.h>
#include <hip/hip_bf16.h>

typedef unsigned short u16;
typedef unsigned long long u64;
using bf16x8 = __attribute__((ext_vector_type(8))) short;
using s16x4 = __attribute__((ext_vector_type(4))) short;
using f32x16 = __attribute__((ext_vector_type(16))) float;
using f32x4 = __attribute__((ext_vector_type(4))) float;
using u32x4 = __attribute__((ext_vector_type(4))) unsigned;
using u32x2 = __attribute__((ext_vector_type(2))) unsigned;
typedef __attribute__((address_space(1))) u64 gu64;

#define DI __device__ __forceinline__
#define RLX_AGENT __ATOMIC_RELAXED, __HIP_MEMORY_SCOPE_AGENT
#define AUX_SC1 16

static constexpr int S_ = 2048;
static constexpr int D_ = 640;
static constexpr int F_ = 1728;

static constexpr unsigned MAT = S_ * D_ * 2;
static constexpr unsigned WS_KVT = 0;
static constexpr unsigned WS_Y2T = MAT;
static constexpr unsigned WS_H = 2 * MAT;
static constexpr unsigned WS_CTL = 3 * MAT;
static constexpr unsigned WS_E = WS_CTL;
static constexpr unsigned WS_STATS = WS_CTL + 4096;
static constexpr unsigned REP_STRIDE = 4352;
static constexpr int R1 = 4;
static constexpr int R2 = 16;
static constexpr int RK = 16;
static constexpr int R4 = 8;
static constexpr unsigned GU_STRIDE = 28160;
static constexpr unsigned WS_F1 = WS_CTL + 32768;
static constexpr unsigned WS_F2 = WS_F1 + R1 * REP_STRIDE;
static constexpr unsigned WS_F3 = WS_F2 + R2 * REP_STRIDE;
static constexpr unsigned WS_KEYS = WS_F3 + (R2 + 8) * REP_STRIDE;
static constexpr unsigned WS_GU = WS_KEYS + RK * REP_STRIDE;
static constexpr unsigned WS_BYTES = WS_GU + R4 * GU_STRIDE;

static constexpr int KC_STRIDE = 4160;
static constexpr int KC_BYTES = 8 * KC_STRIDE;
static constexpr int U_PLANE = 2560;
static constexpr int TEAM = 40960;
static constexpr int L_W4 = 122880;
static constexpr int L_W5 = 81920;
static constexpr int L_TB = 143360;
static constexpr int L_CAND = 154880;
static constexpr int L_RED = 155392;
static constexpr int LDS_BYTES = 163840;

DI float bf2f(unsigned x) { return __uint_as_float(x << 16); }
DI float lo2f(unsigned x) { return __uint_as_float(x << 16); }
DI float hi2f(unsigned x) { return __uint_as_float(x & 0xffff0000u); }
DI unsigned f2bf(float x) {
  __bf16 b = (__bf16)x;
  return (unsigned)__builtin_bit_cast(unsigned short, b);
}
DI float rb(float x) { return (float)((__bf16)x); }
DI unsigned pack2(float lo, float hi) { return f2bf(lo) | (f2bf(hi) << 16); }
typedef float f32x2 __attribute__((ext_vector_type(2)));
typedef __bf16 bf16x2_t __attribute__((ext_vector_type(2)));
DI f32x2 unpk(unsigned x) { return (f32x2){lo2f(x), hi2f(x)}; }
DI unsigned pk(f32x2 v) { return __builtin_bit_cast(unsigned, __builtin_convertvector(v, bf16x2_t)); }
DI f32x2 rb2(f32x2 v) { return unpk(pk(v)); }
DI bool ready4(const u32x4 a) {
  bool ok = true;
#pragma unroll
  for (int d = 0; d < 4; ++d) ok &= ((a[d] & 0xffffu) != 0xffffu) && ((a[d] >> 16) != 0xffffu);
  return ok;
}
DI f32x2 fdiv2(f32x2 a, float b, float rcpb) {
  const f32x2 q0 = a * rcpb;
  const f32x2 e = __builtin_elementwise_fma(-q0, (f32x2){b, b}, a);
  return __builtin_elementwise_fma(e, (f32x2){rcpb, rcpb}, q0);
}

template <int CTRL>
DI int dpp_i(int v) { return __builtin_amdgcn_update_dpp(0, v, CTRL, 0xF, 0xF, true); }
DI float wave_sum(float v) {
  v += __int_as_float(dpp_i<0xB1>(__float_as_int(v)));
  v += __int_as_float(dpp_i<0x4E>(__float_as_int(v)));
  v += __int_as_float(dpp_i<0x141>(__float_as_int(v)));
  v += __int_as_float(dpp_i<0x140>(__float_as_int(v)));
  float a = __int_as_float(__builtin_amdgcn_readlane(__float_as_int(v), 0));
  float b = __int_as_float(__builtin_amdgcn_readlane(__float_as_int(v), 16));
  float c = __int_as_float(__builtin_amdgcn_readlane(__float_as_int(v), 32));
  float d = __int_as_float(__builtin_amdgcn_readlane(__float_as_int(v), 48));
  return (a + b) + (c + d);
}
DI unsigned umax(unsigned a, unsigned b) { return a > b ? a : b; }
DI unsigned wave_umax(unsigned v) {
  v = umax(v, (unsigned)dpp_i<0xB1>((int)v));
  v = umax(v, (unsigned)dpp_i<0x4E>((int)v));
  v = umax(v, (unsigned)dpp_i<0x141>((int)v));
  v = umax(v, (unsigned)dpp_i<0x140>((int)v));
  unsigned a = (unsigned)__builtin_amdgcn_readlane((int)v, 0);
  unsigned b = (unsigned)__builtin_amdgcn_readlane((int)v, 16);
  unsigned c = (unsigned)__builtin_amdgcn_readlane((int)v, 32);
  unsigned d = (unsigned)__builtin_amdgcn_readlane((int)v, 48);
  return umax(umax(a, b), umax(c, d));
}
typedef __attribute__((ext_vector_type(4))) short v4i16_t;
DI s16x4 tr_read(const char* p) {
  return __builtin_bit_cast(s16x4, __builtin_amdgcn_ds_read_tr16_b64_v4i16(
      (__attribute__((address_space(3))) v4i16_t*)p));
}
DI void glds16(const void* gsrc, unsigned lds_dst) {
  unsigned keep;
  asm volatile("s_mov_b32 %0, m0\n\ts_mov_b32 m0, %2\n\ts_nop 0\n\tglobal_load_lds_dwordx4 %1, off\n\ts_mov_b32 m0, %0"
               : "=&s"(keep) : "v"(gsrc), "s"(lds_dst) : "memory");
}
DI void drain() { asm volatile("s_waitcnt vmcnt(0)" ::: "memory"); }
static constexpr int SPIN_CAP = 1 << 20;

template <int MASK>
DI void conv_tiles(const char* kc, const char* us, char* red, int w, int lane) {
  const int rp = lane & 15, g = lane >> 4;
  const char* pa = kc + (rp & 7) * KC_STRIDE + 2 * (2040 - 8 * (rp >> 3) + 8 * g) - 64 * w;
  const char* pb = us + (g >> 1) * U_PLANE + (rp + 16 - w) * 32 + 16 * (g & 1);
  f32x4 acc[4][2];
#pragma unroll
  for (int a = 0; a < 4; ++a) { acc[a][0] = (f32x4){0.f, 0.f, 0.f, 0.f}; acc[a][1] = (f32x4){0.f, 0.f, 0.f, 0.f}; }
#pragma unroll
  for (int mi = 0; mi < 16; ++mi) {
    const int amin = mi >> 2;
    if ((MASK >> amin) == 0) continue;
    const bf16x8 a0 = *(const bf16x8*)(pa - 256 * mi);
    const bf16x8 a1 = *(const bf16x8*)(pa - 256 * mi - 32);
#pragma unroll
    for (int a = 0; a < 4; ++a) {
      if (a >= amin && ((MASK >> a) & 1)) {
        const bf16x8 bb = *(const bf16x8*)(pb + 512 * a - 128 * mi);
        acc[a][0] = __builtin_amdgcn_mfma_f32_16x16x32_bf16(a0, bb, acc[a][0], 0, 0, 0);
        acc[a][1] = __builtin_amdgcn_mfma_f32_16x16x32_bf16(a1, bb, acc[a][1], 0, 0, 0);
      }
    }
  }
  __syncthreads();
  char* rd = red + w * 9216 + rp * 144 + 16 * g;
#pragma unroll
  for (int a = 0; a < 4; ++a) {
    if ((MASK >> a) & 1) {
      *(f32x4*)(rd + a * 16 * 144) = acc[a][0];
      *(f32x4*)(rd + a * 16 * 144 + 64) = acc[a][1];
    }
  }
}

__global__ __launch_bounds__(1024) void fused_kernel(
    const u16* __restrict__ q, const u16* __restrict__ k, const u16* __restrict__ v,
    const u16* __restrict__ shift_k, const u16* __restrict__ ssm_k, const u16* __restrict__ ssm_D,
    const u16* __restrict__ w_r, const u16* __restrict__ w_gate, const u16* __restrict__ w_up,
    const u16* __restrict__ w_down, u16* out, char* ws) {
  __shared__ __attribute__((aligned(16))) char lds[LDS_BYTES];
  asm volatile("" ::"s"(q), "s"(k), "s"(v), "s"(shift_k), "s"(ssm_k), "s"(ssm_D), "s"(w_r), "s"(w_gate), "s"(w_up),
               "s"(w_down), "s"(out), "s"(ws));
  const int b = blockIdx.x;
  const int grp = b & 7, ib = b >> 3;
  const int tid = threadIdx.x, lane = tid & 63;
  const int wave = __builtin_amdgcn_readfirstlane(tid >> 6);
  const int team = wave >> 2, tt = tid & 255;
  const __amdgpu_buffer_rsrc_t rs = __builtin_amdgcn_make_buffer_rsrc(ws, 0, WS_BYTES, 0x00020000);
  const __amdgpu_buffer_rsrc_t ro = __builtin_amdgcn_make_buffer_rsrc(out, 0, MAT, 0x00020000);
  const u32x4 ones = {0xffffffffu, 0xffffffffu, 0xffffffffu, 0xffffffffu};

  const int p1tok = tid / 10, p1chunk = tid - 10 * p1tok;
  const int p1c = 80 * grp + 8 * p1chunk;
  const bool conv_on = team < 3;
  const int chbase = 80 * grp + 5 * (ib >> 1);
  const int odd = ib & 1;
  const int ch = chbase + (team >= 2 ? 2 : (odd ? 3 + team : team));
  const int t0 = (grp * 32 + ib) * 8;
  const unsigned lbase = (unsigned)(size_t)lds;
  u32x4 kown = {0u, 0u, 0u, 0u}, vr = {0u, 0u, 0u, 0u};
  u64 eraw = 0;
  if (wave < 10) {
    const int t = 64 * ib + p1tok;
    kown = *(const u32x4*)(k + (size_t)t * D_ + p1c);
    vr = *(const u32x4*)(v + (size_t)t * D_ + p1c);
    if (wave == 0) eraw = __hip_atomic_load((gu64*)(ws + WS_E), RLX_AGENT);
    *(u32x4*)(lds + (p1tok + 3) * 176 + 16 * p1chunk) = kown;
    asm volatile("" : "+v"(vr), "+v"(eraw));
  } else if (wave == 10) {
    if (lane < 30) {
      const int hr = lane / 10, hc = lane - 10 * hr;
      const int t = 64 * ib - 3 + hr;
      u32x4 kh = {0u, 0u, 0u, 0u};
      if (t >= 0) kh = *(const u32x4*)(k + (size_t)t * D_ + 80 * grp + 8 * hc);
      *(u32x4*)(lds + hr * 176 + 16 * hc) = kh;
    }
    if (b < 216)
      __builtin_amdgcn_raw_buffer_store_b128(ones, rs, WS_GU + (lane >> 3) * GU_STRIDE + ((b & 7) * 27 + (b >> 3)) * 128 + (lane & 7) * 16, 0, AUX_SC1);
  } else if (wave == 11) {
    if (lane < 40) {
      const u32x4 d = *(const u32x4*)(shift_k + (size_t)grp * 320 + 8 * lane);
      *(f32x4*)(lds + 12288 + 32 * lane) = (f32x4){lo2f(d[0]), lo2f(d[2]), hi2f(d[0]), hi2f(d[2])};
      *(f32x4*)(lds + 12288 + 32 * lane + 16) = (f32x4){lo2f(d[1]), lo2f(d[3]), hi2f(d[1]), hi2f(d[3])};
    }
    if (lane < RK) __builtin_amdgcn_raw_buffer_store_b128(ones, rs, WS_KEYS + lane * REP_STRIDE + t0 * 2, 0, AUX_SC1);
  } else if (wave == 15) {
    const char* tp = ws + (size_t)(lane & 31) * (WS_BYTES / 32 & ~15u);
    if (lane >= 32) tp = (const char*)out + (size_t)(lane & 31) * (MAT / 32);
    glds16(tp, lbase + L_RED);
  } else {
    const int chk = chbase + (wave == 14 ? 2 : (odd ? 3 + (wave - 12) : (wave - 12)));
#pragma unroll
    for (int i = 0; i < 4; ++i)
      glds16(ssm_k + (size_t)chk * S_ + 512 * i + 8 * lane, lbase + L_W4 + 4096 * (wave - 12) + 1024 * i);
  }
  __syncthreads();
  unsigned Draw = 0;
  if (wave == 15) {
#pragma unroll
    for (int i = 0; i < 9; ++i) {
      const unsigned o = (unsigned)b * 8640u + i * 1024u + lane * 16u;
      if (i < 8 || lane < 28) {
        glds16((const char*)w_gate + o, lbase + L_RED);
        glds16((const char*)w_up + o, lbase + L_RED);
        glds16((const char*)w_down + o, lbase + L_RED);
      }
    }
  }
  if (wave < 10) {
    u32x4 kr[3];
#pragma unroll
    for (int i = 0; i < 3; ++i) kr[i] = *(const u32x4*)(lds + (p1tok + i) * 176 + 16 * p1chunk);
#pragma unroll
    for (int jp = 0; jp < 4; ++jp) {
      const f32x4 wa = *(const f32x4*)(lds + 12288 + 128 * p1chunk + 32 * jp);
      const f32x4 wb = *(const f32x4*)(lds + 12288 + 128 * p1chunk + 32 * jp + 16);
      const f32x2 w0 = {wa[0], wa[1]}, w1 = {wa[2], wa[3]}, w2 = {wb[0], wb[1]}, w3 = {wb[2], wb[3]};
      f32x2 ks = rb2(unpk(kr[0][jp]) * w3);
      ks = rb2(ks + rb2(unpk(kr[1][jp]) * w2));
      ks = rb2(ks + rb2(unpk(kr[2][jp]) * w1));
      ks = rb2(ks + rb2(unpk(kown[jp]) * w0));
      const unsigned o = pk(ks * unpk(vr[jp]));
      char* dst = lds + L_TB + (8 * p1chunk + 2 * jp) * 128 + 16 * p1chunk + 2 * p1tok;
      *(u16*)dst = (u16)(o & 0xffffu);
      *(u16*)(dst + 128) = (u16)(o >> 16);
    }
  }
  asm volatile("" : "+v"(eraw));
  const u64 tag0 = eraw + 1ull;
  if (tid == 0) { *(u64*)(lds + L_CAND + 384) = tag0; *(unsigned*)(lds + L_CAND + 400) = 0u; }
  if (wave >= 12 && wave < 15) drain();
  __syncthreads();
  if (tid < 640) {
    const int row = tid >> 3, piece = tid & 7;
    const u32x4 d = *(const u32x4*)(lds + L_TB + row * 128 + 16 * (row >> 3) + 16 * piece);
    __builtin_amdgcn_raw_buffer_store_b128(d, rs, WS_KVT + ((80 * grp + row) * S_ + 64 * ib + 8 * piece) * 2, 0, AUX_SC1);
  }
  const u64 tag = *(const u64*)(lds + L_CAND + 384);
  char* kc = lds + TEAM * team;
  char* us = kc + KC_BYTES;
  if (conv_on) {
    const u32x4 z4 = {0u, 0u, 0u, 0u};
    u32x4 klo = *(const u32x4*)(lds + L_W4 + 4096 * team + 16 * (tt < 255 ? 254 - tt : 0));
    const u32x4 khi = *(const u32x4*)(lds + L_W4 + 4096 * team + 16 * (255 - tt));
    if (tt >= 255) klo = z4;
    if (tt < 64) *(u32x4*)(us + (tt >> 5) * U_PLANE + 16 * (tt & 31)) = z4;
    if (tt < 32) *(u32x4*)(kc + (tt >> 2) * KC_STRIDE + 16 * (256 + (tt & 3))) = z4;
    const unsigned W[8] = {klo[0], klo[1], klo[2], klo[3], khi[0], khi[1], khi[2], khi[3]};
    unsigned E[7], O[7];
#pragma unroll
    for (int d = 0; d < 7; ++d) {
      E[d] = (W[7 - d] >> 16) | (W[7 - d] << 16);
      O[d] = (W[7 - d] & 0xffffu) | (W[6 - d] & 0xffff0000u);
    }
    char* dst = kc + 16 * tt;
#pragma unroll
    for (int qq = 0; qq < 4; ++qq) {
      *(u32x4*)(dst + (7 - 2 * qq) * KC_STRIDE) = (u32x4){E[qq], E[qq + 1], E[qq + 2], E[qq + 3]};
      *(u32x4*)(dst + (6 - 2 * qq) * KC_STRIDE) = (u32x4){O[qq], O[qq + 1], O[qq + 2], O[qq + 3]};
    }
  }
  if (wave < 12) drain();
  __syncthreads();
  if (tid < R1) __hip_atomic_store((gu64*)(ws + WS_F1 + tid * REP_STRIDE) + grp * 32 + ib, tag, RLX_AGENT);

  unsigned xr[5], wr[5];
  if (wave < 8) {
    const int t = t0 + wave;
#pragma unroll
    for (int i = 0; i < 5; ++i) xr[i] = *(const unsigned*)(q + (size_t)t * D_ + 2 * lane + 128 * i);
#pragma unroll
    for (int i = 0; i < 5; ++i) wr[i] = *(const unsigned*)(w_r + 2 * lane + 128 * i);
  }

  if (wave == 12) {
    const gu64* f = (const gu64*)(ws + WS_F1 + (ib % R1) * REP_STRIDE) + grp * 32 + (lane & 31);
    for (int it = 0; it < SPIN_CAP; ++it) {
      const bool ok = __hip_atomic_load(f, RLX_AGENT) == tag;
      if (__all(ok)) break;
      __builtin_amdgcn_s_sleep(1);
    }
  }
  __syncthreads();
  u32x4 uu = {0u, 0u, 0u, 0u};
  if (conv_on) {
    Draw = ssm_D[ch];
    uu = __builtin_amdgcn_raw_buffer_load_b128(rs, WS_KVT + (ch * S_ + 8 * tt) * 2, 0, AUX_SC1);
    *(u32x4*)(us + ((tt >> 1) & 1) * U_PLANE + ((tt >> 2) + 16) * 32 + 16 * (tt & 1)) = uu;
  }
  __syncthreads();
  if (b < 216) {
    if (wave >= 12) {
      const int f0 = ((b & 7) * 27 + (b >> 3)) * 8;
#pragma unroll
      for (int i = 0; i < 5; ++i) {
        const int p = tt + 256 * i;
        const u16* src = ((p & 1) ? w_up : w_gate) + (size_t)(p >> 1) * F_ + f0;
        glds16(src, lbase + L_W4 + 16 * ((wave - 12) * 64 + 256 * i));
      }
    }
  } else if (wave >= 12) {
    const int j5 = b - 216;
    const int c05 = 16 * ((j5 & 7) * 5 + (j5 >> 3));
    for (int rb0 = (wave - 12) * 64; rb0 < F_; rb0 += 256)
      glds16(w_down + (size_t)(rb0 + lane) * D_ + c05, lbase + L_RED);
  }
  if (team < 2) conv_tiles<15>(kc, us, kc, wave & 3, lane);
  else if (team == 2) { if (odd) conv_tiles<6>(kc, us, kc, wave & 3, lane); else conv_tiles<9>(kc, us, kc, wave & 3, lane); }
  else __syncthreads();
  __syncthreads();
  const int myal = tt >> 6;
  if (team < 2 || (team == 2 && ((myal == 1 || myal == 2) == (odd != 0)))) {
    const char* red = kc + (tt >> 2) * 144 + (tt & 3) * 32;
    f32x4 s0 = *(const f32x4*)(red), s1 = *(const f32x4*)(red + 16);
#pragma unroll
    for (int w = 1; w < 4; ++w) {
      s0 += *(const f32x4*)(red + w * 9216);
      s1 += *(const f32x4*)(red + w * 9216 + 16);
    }
    const float Dc = bf2f(Draw);
    const u32x4 st = {pk(rb2((f32x2){s0[0], s0[1]}) + rb2(unpk(uu[0]) * Dc)), pk(rb2((f32x2){s0[2], s0[3]}) + rb2(unpk(uu[1]) * Dc)),
                      pk(rb2((f32x2){s1[0], s1[1]}) + rb2(unpk(uu[2]) * Dc)), pk(rb2((f32x2){s1[2], s1[3]}) + rb2(unpk(uu[3]) * Dc))};
    __builtin_amdgcn_raw_buffer_store_b128(st, rs, WS_Y2T + (ch * S_ + 8 * tt) * 2, 0, AUX_SC1);
  }
  if (conv_on) drain();
  __syncthreads();
  if (tid < R2) __hip_atomic_store((gu64*)(ws + WS_F2 + tid * REP_STRIDE) + b, tag, RLX_AGENT);
  if (b >= 216 && wave >= 13) {
    const int j5 = b - 216;
    const int c05 = 16 * ((j5 & 7) * 5 + (j5 >> 3));
    for (int pb = (wave - 13) * 64; pb < 3456; pb += 192) {
      const int p = pb + lane;
      glds16(w_down + (size_t)(p >> 1) * D_ + c05 + 8 * (p & 1), lbase + L_W5 + 16 * pb);
    }
  }

  f32x2 xf[5], ql[5], wf[5];
  if (wave < 8) {
#pragma unroll
    for (int i = 0; i < 5; ++i) { xf[i] = unpk(xr[i]); wf[i] = unpk(wr[i]); }
    const f32x2 s2 = ((xf[0] + xf[1]) + (xf[2] + xf[3])) + xf[4];
    const float mean1 = wave_sum(s2[0] + s2[1]) * (1.0f / D_);
    f32x2 q2 = {0.f, 0.f};
#pragma unroll
    for (int i = 0; i < 5; ++i) { const f32x2 d = xf[i] - mean1; q2 += d * d; }
    const float var1 = wave_sum(q2[0] + q2[1]) * (1.0f / D_);
    const float m1 = rb(mean1);
    const float sd1 = rb(sqrtf(rb(rb(var1) + 1e-5f)));
    const float rc1 = __builtin_amdgcn_rcpf(sd1);
#pragma unroll
    for (int i = 0; i < 5; ++i) ql[i] = rb2(fdiv2(rb2(xf[i] - m1), sd1, rc1));
    asm volatile("" : "+v"(ql[0]), "+v"(ql[1]), "+v"(ql[2]), "+v"(ql[3]), "+v"(ql[4]), "+v"(wf[0]), "+v"(wf[1]),
                 "+v"(wf[2]), "+v"(wf[3]), "+v"(wf[4]));
  }

  if (wave == 11) {
    const gu64* f = (const gu64*)(ws + WS_F2 + (b % R2) * REP_STRIDE) + lane;
    for (int it = 0; it < SPIN_CAP; ++it) {
      bool ok = true;
#pragma unroll
      for (int i = 0; i < 4; ++i) ok &= __hip_atomic_load(f + 64 * i, RLX_AGENT) == tag;
      if (__all(ok)) break;
      __builtin_amdgcn_s_sleep(1);
    }
  }
  __syncthreads();
  {
    u16* ys = (u16*)(lds + L_TB);
    if (wave < 10) {
      const u32x4 a = __builtin_amdgcn_raw_buffer_load_b128(rs, WS_Y2T + (tid * S_ + t0) * 2, 0, AUX_SC1);
#pragma unroll
      for (int j = 0; j < 8; ++j) ys[j * 648 + tid] = (u16)((j & 1) ? (a[j >> 1] >> 16) : (a[j >> 1] & 0xffffu));
    }
    __syncthreads();
    f32x2 hf[5], hn[5];
    float m2 = 0.f, sd2 = 1.f;
    if (wave < 8) {
#pragma unroll
      for (int i = 0; i < 5; ++i) {
        const unsigned yy = *(const unsigned*)&ys[wave * 648 + 2 * lane + 128 * i];
        hf[i] = rb2(xf[i] + rb2(ql[i] * unpk(yy)));
      }
      const f32x2 s2 = ((hf[0] + hf[1]) + (hf[2] + hf[3])) + hf[4];
      const float mean2 = wave_sum(s2[0] + s2[1]) * (1.0f / D_);
      f32x2 q2 = {0.f, 0.f};
#pragma unroll
      for (int i = 0; i < 5; ++i) { const f32x2 d = hf[i] - mean2; q2 += d * d; }
      const float var2 = wave_sum(q2[0] + q2[1]) * (1.0f / D_);
      m2 = rb(mean2);
      sd2 = rb(sqrtf(rb(rb(var2) + 1e-5f)));
      const float rc2 = __builtin_amdgcn_rcpf(sd2);
      f32x2 d2 = {0.f, 0.f};
#pragma unroll
      for (int i = 0; i < 5; ++i) {
        hn[i] = rb2(fdiv2(rb2(hf[i] - m2), sd2, rc2));
        d2 += hn[i] * wf[i];
      }
      const float dot = d2[0] + d2[1];
      const float rr = wave_sum(dot);
      unsigned rbits = f2bf(rr);
      if (rbits == 0xffffu) rbits = 0xfffeu;
      if (lane == 0) ((u16*)(lds + L_CAND))[wave] = (u16)rbits;
    }
    __syncthreads();
    if (wave == 8 && lane < RK) {
      const u32x4 kq = *(const u32x4*)(lds + L_CAND);
      __builtin_amdgcn_raw_buffer_store_b128(kq, rs, WS_KEYS + lane * REP_STRIDE + t0 * 2, 0, AUX_SC1);
    }
    if (wave < 8) {
      const int t = t0 + wave;
      unsigned* stg = (unsigned*)(lds + wave * 2560);
#pragma unroll
      for (int i = 0; i < 5; ++i) {
        stg[lane + 64 * i] = pk(hf[i] + hn[i]);
        stg[320 + lane + 64 * i] = pk(hf[i]);
      }
      __builtin_amdgcn_fence(__ATOMIC_RELEASE, "wavefront");
      __builtin_amdgcn_wave_barrier();
      __builtin_amdgcn_fence(__ATOMIC_ACQUIRE, "wavefront");
      const u32x4 h0 = *(const u32x4*)(stg + 320 + 4 * lane);
      __builtin_amdgcn_raw_buffer_store_b128(h0, rs, WS_H + (t * D_ + 8 * lane) * 2, 0, AUX_SC1);
      if (lane < 16) {
        const u32x4 h1 = *(const u32x4*)(stg + 320 + 256 + 4 * lane);
        __builtin_amdgcn_raw_buffer_store_b128(h1, rs, WS_H + (t * D_ + 512 + 8 * lane) * 2, 0, AUX_SC1);
      }
      if (lane == 0) {
        const u32x2 sv = {__float_as_uint(m2), __float_as_uint(sd2)};
        __builtin_amdgcn_raw_buffer_store_b64(sv, rs, WS_STATS + t * 8, 0, AUX_SC1);
      }
    }
    drain();
    if (wave < 8) {
      unsigned old = 0;
      if (lane == 0) old = atomicAdd((unsigned*)(lds + L_CAND + 400), 1u);
      old = __builtin_amdgcn_readfirstlane(old);
      if (old == 7u && lane < R2) __hip_atomic_store((gu64*)(ws + WS_F3 + lane * REP_STRIDE) + b, tag, RLX_AGENT);
    }
  }

  unsigned* cand = (unsigned*)(lds + L_CAND);
  unsigned kk[8];
  const bool tkw = (wave >= 8 && wave < 12);
  const int tkt = tid - 512;
  if (tkw) {
    u32x4 gq = ones;
    __builtin_amdgcn_s_sleep(24);
    for (int it = 0; it < SPIN_CAP; ++it) {
      gq = __builtin_amdgcn_raw_buffer_load_b128(rs, WS_KEYS + (b % RK) * REP_STRIDE + tkt * 16, 0, AUX_SC1);
      if (__all(ready4(gq))) break;
      __builtin_amdgcn_s_sleep(10);
    }
#pragma unroll
    for (int j = 0; j < 8; ++j) {
      unsigned x = (j & 1) ? (gq[j >> 1] >> 16) : (gq[j >> 1] & 0xffffu);
      x = (x & 0x8000u) ? (~x & 0xffffu) : (x | 0x8000u);
      kk[j] = (x << 16) | (unsigned)(0xffff - 8 * tkt - j);
    }
    unsigned m = kk[0];
#pragma unroll
    for (int j = 1; j < 8; ++j) m = umax(m, kk[j]);
    const unsigned M1 = wave_umax(m);
    m = 0;
#pragma unroll
    for (int j = 0; j < 8; ++j) m = umax(m, kk[j] == M1 ? 0u : kk[j]);
    const unsigned M2 = wave_umax(m);
    if (lane == 0) { cand[64 + wave - 8] = M2; if (wave == 8) cand[68] = 0; }
  }
  __syncthreads();
  if (tkw) {
    const unsigned th = min(min(cand[64], cand[65]), min(cand[66], cand[67]));
#pragma unroll
    for (int j = 0; j < 8; ++j) {
      if (kk[j] >= th) {
        const unsigned pos = atomicAdd(&cand[68], 1u);
        if (pos < 64) cand[pos] = kk[j];
      }
    }
  }
  __syncthreads();
  if (wave == 12) {
    const gu64* f = (const gu64*)(ws + WS_F3 + (b % R2) * REP_STRIDE) + lane;
    for (int it = 0; it < SPIN_CAP; ++it) {
      bool ok = true;
#pragma unroll
      for (int i = 0; i < 4; ++i) ok &= __hip_atomic_load(f + 64 * i, RLX_AGENT) == tag;
      if (__all(ok)) break;
      __builtin_amdgcn_s_sleep(1);
    }
  }
  const unsigned ncand = cand[68];
  if (ncand <= 64) {
    if (wave == 0) {
      const unsigned mine = (lane < (int)ncand) ? cand[lane] : 0u;
      unsigned rank = 0;
      for (unsigned i = 0; i < ncand; ++i) rank += (cand[i] > mine) ? 1u : 0u;
      if (lane < (int)ncand && rank < 8) cand[72 + rank] = mine;
    }
  } else {
    for (int rnd = 0; rnd < 8; ++rnd) {
      __syncthreads();
      if (tkw) {
        unsigned m = kk[0];
#pragma unroll
        for (int j = 1; j < 8; ++j) m = umax(m, kk[j]);
        const unsigned M = wave_umax(m);
        if (lane == 0) cand[64 + wave - 8] = M;
      }
      __syncthreads();
      const unsigned M = umax(umax(cand[64], cand[65]), umax(cand[66], cand[67]));
      if (tkw) {
#pragma unroll
        for (int j = 0; j < 8; ++j) kk[j] = (kk[j] == M) ? 0u : kk[j];
      }
      if (tid == 0) cand[72 + rnd] = M;
    }
  }
  __syncthreads();
  if (wave >= 10) {
    for (int rr = wave - 10; rr < 8; rr += 6) {
      const int t = t0 + rr;
      bool routed = false;
#pragma unroll
      for (int j = 0; j < 8; ++j) routed |= (0xffff - (int)(cand[72 + j] & 0xffffu)) == t;
      if (!routed) {
        const unsigned* stg = (const unsigned*)(lds + rr * 2560);
        __builtin_amdgcn_raw_buffer_store_b128(*(const u32x4*)(stg + 4 * lane), ro, (t * D_ + 8 * lane) * 2, 0, AUX_SC1);
        if (lane < 16) __builtin_amdgcn_raw_buffer_store_b128(*(const u32x4*)(stg + 256 + 4 * lane), ro, (t * D_ + 512 + 8 * lane) * 2, 0, AUX_SC1);
      }
    }
  }
  const int tok = lane & 15, g = lane >> 4, i16 = lane & 15;
  float* red = (float*)(lds + L_RED);
  if (b < 216) {
    const int slab = (b & 7) * 27 + (b >> 3);
    const int f0 = slab * 8;
    if (wave < 10) {
      const unsigned mykey = cand[72 + (tok & 7)];
      const int myidx = 0xffff - (int)(mykey & 0xffffu);
      const u32x2 stv = __builtin_amdgcn_raw_buffer_load_b64(rs, WS_STATS + myidx * 8, 0, AUX_SC1);
      u32x4 hraw[2];
#pragma unroll
      for (int i = 0; i < 2; ++i)
        hraw[i] = __builtin_amdgcn_raw_buffer_load_b128(rs, WS_H + (myidx * D_ + 32 * (2 * wave + i) + 8 * g) * 2, 0, AUX_SC1);
      const float sm = __uint_as_float(stv[0]), ss = __uint_as_float(stv[1]);
      const float rcs = __builtin_amdgcn_rcpf(ss);
      f32x4 acc = {0.f, 0.f, 0.f, 0.f};
#pragma unroll
      for (int i = 0; i < 2; ++i) {
        const int k0 = 32 * (2 * wave + i);
        u32x4 HN;
#pragma unroll
        for (int d = 0; d < 4; ++d) {
          HN[d] = pk(fdiv2(rb2(unpk(hraw[i][d]) - sm), ss, rcs));
        }
        const char* p = lds + L_W4 + (k0 + 8 * g + (i16 >> 2)) * 32 + 8 * (i16 & 3);
        const s16x4 blo = tr_read(p);
        const s16x4 bhi = tr_read(p + 4 * 32);
        const bf16x8 bfrag = __builtin_shufflevector(blo, bhi, 0, 1, 2, 3, 4, 5, 6, 7);
        acc = __builtin_amdgcn_mfma_f32_16x16x32_bf16(__builtin_bit_cast(bf16x8, HN), bfrag, acc, 0, 0, 0);
      }
      if (g < 2) {
#pragma unroll
        for (int j = 0; j < 4; ++j) red[(wave * 8 + 4 * g + j) * 16 + i16] = acc[j];
      }
    }
    __syncthreads();
    if (wave == 0) {
      const int tk = lane >> 3, col = lane & 7;
      float G = 0.f, U = 0.f;
#pragma unroll
      for (int w = 0; w < 10; ++w) {
        G += red[(w * 8 + tk) * 16 + col];
        U += red[(w * 8 + tk) * 16 + col + 8];
      }
      G = rb(G); U = rb(U);
      const float gg = rb(G / (1.0f + __expf(-G)));
      u16* stg = (u16*)(lds + L_CAND);
      unsigned gub = f2bf(gg * U);
      if (gub == 0xffffu) gub = 0xfffeu;
      stg[lane] = (u16)gub;
      __builtin_amdgcn_fence(__ATOMIC_RELEASE, "wavefront");
      __builtin_amdgcn_wave_barrier();
      __builtin_amdgcn_fence(__ATOMIC_ACQUIRE, "wavefront");
      {
        const u32x4 o = *(const u32x4*)(stg + 8 * (lane & 7));
        __builtin_amdgcn_raw_buffer_store_b128(o, rs, WS_GU + (lane >> 3) * GU_STRIDE + slab * 128 + (lane & 7) * 16, 0, AUX_SC1);
      }
    }
  } else {
    const int j = b - 216;
    const int c0 = 16 * ((j & 7) * 5 + (j >> 3));
    unsigned key = 0, hv = 0;
    u32x2 stv = {0u, 0u};
    if (wave < 2) {
      const int tk = tid >> 4;
      key = cand[72 + tk];
      const int idx = 0xffff - (int)(key & 0xffffu);
      hv = (unsigned)(u16)__builtin_amdgcn_raw_buffer_load_b16(rs, WS_H + (idx * D_ + c0 + (tid & 15)) * 2, 0, AUX_SC1);
      stv = __builtin_amdgcn_raw_buffer_load_b64(rs, WS_STATS + idx * 8, 0, AUX_SC1);
    }
    {
      u32x4 af[4];
      __builtin_amdgcn_s_sleep(40);
      for (int it = 0; it < SPIN_CAP; ++it) {
        bool ok = true;
#pragma unroll
        for (int i = 0; i < 4; ++i) {
          const int ks = wave + 16 * i;
          const int ksc = ks < 54 ? ks : 0;
          af[i] = (u32x4){0u, 0u, 0u, 0u};
          if (tok < 8) af[i] = __builtin_amdgcn_raw_buffer_load_b128(rs, WS_GU + (j % R4) * GU_STRIDE + (4 * ksc + g) * 128 + tok * 16, 0, AUX_SC1);
        }
#pragma unroll
        for (int i = 0; i < 4; ++i) ok &= ready4(af[i]);
        if (__all(ok)) break;
        __builtin_amdgcn_s_sleep(2);
      }
      f32x4 acc = {0.f, 0.f, 0.f, 0.f};
#pragma unroll
      for (int i = 0; i < 4; ++i) {
        const int ks = wave + 16 * i;
        if (ks < 54) {
          const int k0 = 32 * ks;
          const char* p = lds + L_W5 + (k0 + 8 * g + (i16 >> 2)) * 32 + 8 * (i16 & 3);
          const s16x4 blo = tr_read(p);
          const s16x4 bhi = tr_read(p + 4 * 32);
          const bf16x8 bfrag = __builtin_shufflevector(blo, bhi, 0, 1, 2, 3, 4, 5, 6, 7);
          acc = __builtin_amdgcn_mfma_f32_16x16x32_bf16(__builtin_bit_cast(bf16x8, af[i]), bfrag, acc, 0, 0, 0);
        }
      }
      if (g < 2) {
#pragma unroll
        for (int jj = 0; jj < 4; ++jj) red[(wave * 8 + 4 * g + jj) * 16 + i16] = acc[jj];
      }
    }
    __syncthreads();
    if (wave < 2) {
      const int tk = tid >> 4, col = tid & 15;
      float Y = 0.f;
#pragma unroll
      for (int w = 0; w < 16; ++w) Y += red[(w * 8 + tk) * 16 + col];
      Y = rb(Y);
      const unsigned ord = key >> 16;
      const unsigned rbits = (ord & 0x8000u) ? (ord & 0x7fffu) : (~ord & 0xffffu);
      const float rv = bf2f(rbits);
      const float rsg = rb(1.0f / (1.0f + __expf(-rv)));
      const float y = rb(Y * rsg);
      const float hval = bf2f(hv);
      const float hn = rb(rb(hval - __uint_as_float(stv[0])) / __uint_as_float(stv[1]));
      const float bo = rb(hn + y);
      const int idx = 0xffff - (int)(key & 0xffffu);
      __builtin_amdgcn_raw_buffer_store_b16((short)f2bf(hval + bo), ro, (idx * D_ + c0 + col) * 2, 0, AUX_SC1);
    }
    if (b == 216 && tid == 0) __hip_atomic_store((gu64*)(ws + WS_E), tag, RLX_AGENT);
  }
}

extern "C" void kernel_launch(void* const* d_in, const int* in_sizes, int n_in, void* d_out, int out_size,
                              void* d_ws, size_t ws_size, hipStream_t stream) {
  fused_kernel<<<256, 1024, 0, stream>>>(
      (const u16*)d_in[0], (const u16*)d_in[1], (const u16*)d_in[2], (const u16*)d_in[3], (const u16*)d_in[4],
      (const u16*)d_in[5], (const u16*)d_in[7], (const u16*)d_in[8], (const u16*)d_in[9], (const u16*)d_in[10],
      (u16*)d_out, (char*)d_ws);
}
```
